```python
import torch

def _softmax(x, axis=-1):
    return torch.softmax(x, dim=axis)
def _gelu(x):
    return torch.nn.functional.gelu(x, approximate="tanh")
def _silu(x):
    return torch.nn.functional.silu(x)
def _sigmoid(x):
    return torch.sigmoid(x)
def _layernorm(x, axis=-1, eps=1e-5):
    m = x.mean(dim=axis, keepdim=True)
    v = x.var(dim=axis, keepdim=True, unbiased=False)
    return (x - m) / torch.sqrt(v + eps)
def _scatter_add(tgt, idx, src, axis=0):
    return tgt.clone().index_add_(axis, idx, src)
def _segment_sum(src, seg, n):
    out = torch.zeros((n,) + src.shape[1:], dtype=src.dtype)
    return out.index_add_(0, seg, src)
def _pad(x, pw):
    flat = tuple(v for pair in reversed(pw) for v in pair)
    return torch.nn.functional.pad(x, flat)

kernel_name = 'standalone__gqa_softmax__near_ridge'

def setup_inputs():

    _w = (lambda a: (lambda t: t.to(torch.bfloat16) if t.is_floating_point() else t)(torch.as_tensor(a)))
    B, Hq, Hkv, Sq, Sk, D = 8, 16, 8, 384, 512, 256
    return {
        "q": _w((lambda *s: torch.randn(*s, dtype=torch.bfloat16) * 0.8)(B, Hq, Sq, D)),
        "k": _w((lambda *s: torch.randn(*s, dtype=torch.bfloat16) * 0.8)(B, Hkv, Sk, D)),
        "v": _w((lambda *s: torch.randn(*s, dtype=torch.bfloat16) * 0.05)(B, Hkv, Sk, D)),
    }

def reference(q, k, v):
    B, Hq, Sq, D = q.shape
    Hkv = k.shape[1]
    rep = Hq // Hkv
    k = (lambda x, n, axis: torch.repeat_interleave(x, n, dim=axis))(k, rep, axis=1)
    v = (lambda x, n, axis: torch.repeat_interleave(x, n, dim=axis))(v, rep, axis=1)
    scale = D ** -0.5
    scores = torch.einsum("bhqd,bhkd->bhqk", q, k) * scale
    p = _softmax(scores, axis=-1)
    return torch.einsum("bhqk,bhkd->bhqd", p, v)
```

```cpp
#include <hip/hip_runtime.h>
#include <hip/hip_bf16.h>

using bf16x8 = __attribute__((ext_vector_type(8))) short;
using s16x4  = __attribute__((ext_vector_type(4))) short;
using f32x16 = __attribute__((ext_vector_type(16))) float;
using u32x4  = __attribute__((ext_vector_type(4))) unsigned;
using u32x2  = __attribute__((ext_vector_type(2))) unsigned;
typedef float f32x2 __attribute__((ext_vector_type(2)));
typedef __bf16 bf16x2 __attribute__((ext_vector_type(2)));

constexpr int B_ = 8, HQ = 16, HKV = 8, SQ = 384, SK = 512, D = 256;
constexpr int ROWS = 192, KT = 32;
constexpr int RS = 528;

__device__ __forceinline__ unsigned cvtpk(float lo, float hi) {
  f32x2 v = {lo, hi}; bf16x2 b = __builtin_convertvector(v, bf16x2); return __builtin_bit_cast(unsigned, b);
}
template <int OFF> __device__ __forceinline__ s16x4 tr_read(unsigned a) { s16x4 r; asm volatile("ds_read_b64_tr_b16 %0, %1 offset:%2\n\ts_waitcnt lgkmcnt(0)" : "=&v"(r) : "v"(a), "i"(OFF) : "memory"); return r; }
template <int C, int KS> __device__ __forceinline__ void pv1(f32x16& o, unsigned vb, bf16x8 pf) {
  s16x4 lo = tr_read<(16 * KS) * RS + 64 * C>(vb);
  s16x4 hi = tr_read<(16 * KS + 8) * RS + 64 * C>(vb);
  bf16x8 vf = __builtin_shufflevector(lo, hi, 0, 1, 2, 3, 4, 5, 6, 7);
  o = __builtin_amdgcn_mfma_f32_32x32x16_bf16(vf, pf, o, 0, 0, 0);
}

__global__ __launch_bounds__(384, 1)
void attn_base(const short* __restrict__ Q, const short* __restrict__ K, const short* __restrict__ V, short* __restrict__ O) {
  __shared__ __attribute__((aligned(16))) char lds[2 * KT * RS];
  char* Ks = lds; char* Vs = lds + KT * RS;
  const int tid = threadIdx.x, wid = tid >> 6, lane = tid & 63, r = lane & 31, h = lane >> 5;
  const int bid = blockIdx.x;
  const int grp = (bid & 7) * 8 + ((bid >> 3) >> 2);
  const int sub = (bid >> 3) & 3;
  const long qrow0 = (long)grp * 768 + sub * 192 + wid * 32;
  const short* Kg = K + (long)grp * SK * D;
  const short* Vg = V + (long)grp * SK * D;
  bf16x8 qf[16];
#pragma unroll
  for (int s = 0; s < 16; ++s) qf[s] = *(const bf16x8*)(Q + (qrow0 + r) * D + s * 16 + h * 8);
  f32x16 o[8];
#pragma unroll
  for (int c = 0; c < 8; ++c) o[c] = f32x16{};
  float m = -1e30f, l = 0.f;
  const float C = 0.0625f * 1.4426950408889634f;
  const unsigned vbase = (unsigned)(size_t)Vs;
  const int blk = (lane >> 4) & 1, q4 = (lane & 15) >> 2, p4 = lane & 3;
  const unsigned vb = vbase + (4 * h + q4) * RS + (16 * blk + 4 * p4) * 2;
  for (int t = 0; t < SK / KT; ++t) {
    __syncthreads();
    for (int i = tid; i < 2048; i += 384) {
      int which = i >> 10, c = i & 1023, row = c >> 5, ch = c & 31;
      const short* src = (which ? Vg : Kg) + (long)(t * KT + row) * D + ch * 8;
      *(bf16x8*)(lds + which * KT * RS + row * RS + ch * 16) = *(const bf16x8*)src;
    }
    __syncthreads();
    f32x16 s = {};
#pragma unroll
    for (int k = 0; k < 16; ++k) {
      bf16x8 kf = *(const bf16x8*)(Ks + r * RS + k * 32 + h * 16);
      s = __builtin_amdgcn_mfma_f32_32x32x16_bf16(kf, qf[k], s, 0, 0, 0);
    }
    float mx = s[0];
#pragma unroll
    for (int i = 1; i < 16; ++i) mx = fmaxf(mx, s[i]);
    mx = fmaxf(mx, __shfl_xor(mx, 32));
    float mn = fmaxf(m, mx);
    float alpha = __builtin_amdgcn_exp2f((m - mn) * C);
    m = mn;
    float ps = 0.f;
#pragma unroll
    for (int i = 0; i < 16; ++i) { s[i] = __builtin_amdgcn_exp2f((s[i] - mn) * C); ps += s[i]; }
    ps += __shfl_xor(ps, 32);
    l = l * alpha + ps;
#pragma unroll
    for (int c = 0; c < 8; ++c)
#pragma unroll
      for (int i = 0; i < 16; ++i) o[c][i] *= alpha;
    bf16x8 pf[2];
#pragma unroll
    for (int ks = 0; ks < 2; ++ks) {
      u32x4 w = {cvtpk(s[8*ks], s[8*ks+1]), cvtpk(s[8*ks+2], s[8*ks+3]), cvtpk(s[8*ks+4], s[8*ks+5]), cvtpk(s[8*ks+6], s[8*ks+7])};
      pf[ks] = __builtin_bit_cast(bf16x8, w);
    }
#define PV(C) pv1<C,0>(o[C], vb, pf[0]); pv1<C,1>(o[C], vb, pf[1]);
    PV(0) PV(1) PV(2) PV(3) PV(4) PV(5) PV(6) PV(7)
  }
  const float il = 1.f / l;
  short* Orow = O + (qrow0 + r) * D;
#pragma unroll
  for (int c = 0; c < 8; ++c)
#pragma unroll
    for (int g = 0; g < 4; ++g) {
      u32x2 w = {cvtpk(o[c][4*g] * il, o[c][4*g+1] * il), cvtpk(o[c][4*g+2] * il, o[c][4*g+3] * il)};
      *(u32x2*)(Orow + 32 * c + 8 * g + 4 * h) = w;
    }
}

extern "C" void kernel_launch(void* const* d_in, const int* in_sizes, int n_in,
                              void* d_out, int out_size, void* d_ws, size_t ws_size,
                              hipStream_t stream) {
  attn_base<<<256, 384, 0, stream>>>((const short*)d_in[0], (const short*)d_in[1], (const short*)d_in[2], (short*)d_out);
}
```

```cpp
#include <hip/hip_runtime.h>
#include <hip/hip_bf16.h>

using bf16x8 = __attribute__((ext_vector_type(8))) short;
using s16x4  = __attribute__((ext_vector_type(4))) short;
using f32x16 = __attribute__((ext_vector_type(16))) float;
using u32x4  = __attribute__((ext_vector_type(4))) unsigned;
using u32x2  = __attribute__((ext_vector_type(2))) unsigned;
typedef float f32x2 __attribute__((ext_vector_type(2)));
typedef __bf16 bf16x2 __attribute__((ext_vector_type(2)));

constexpr int SK = 512, D = 256, KT = 32, RS = 528;
constexpr unsigned QBYTES = 8u * 16u * 384u * 512u, KBYTES = 8u * 8u * 512u * 512u;

__device__ __forceinline__ unsigned cvtpk(float lo, float hi) {
  f32x2 v = {lo, hi}; bf16x2 b = __builtin_convertvector(v, bf16x2); return __builtin_bit_cast(unsigned, b);
}
template <int OFF> __device__ __forceinline__ s16x4 tr_read(unsigned a) {
  s16x4 r; asm volatile("ds_read_b64_tr_b16 %0, %1 offset:%2\n\ts_waitcnt lgkmcnt(0)" : "=&v"(r) : "v"(a), "i"(OFF) : "memory"); return r;
}
template <int C, int KS> __device__ __forceinline__ void pv1(f32x16& o, unsigned vb, bf16x8 pf) {
  s16x4 lo = tr_read<(16 * KS) * RS + 64 * C>(vb);
  s16x4 hi = tr_read<(16 * KS + 8) * RS + 64 * C>(vb);
  bf16x8 vf = __builtin_shufflevector(lo, hi, 0, 1, 2, 3, 4, 5, 6, 7);
  o = __builtin_amdgcn_mfma_f32_32x32x16_bf16(vf, pf, o, 0, 0, 0);
}

__device__ __forceinline__ void slow_path(const char* Qc, const char* Kc, const char* Vc, char* Oc, char* lds, int grp, int sub, int wid) {
  const int lane = __builtin_amdgcn_mbcnt_hi(-1, __builtin_amdgcn_mbcnt_lo(-1, 0));
  const int tid = wid * 64 + lane, r = lane & 31, h = lane >> 5;
  const short* Q = (const short*)Qc; short* O = (short*)Oc;
  const short* Kg = (const short*)Kc + (long)grp * SK * D;
  const short* Vg = (const short*)Vc + (long)grp * SK * D;
  char* Ks = lds;
  const float C = 0.0625f * 1.4426950408889634f;
  const int blk = (lane >> 4) & 1, q4 = (lane & 15) >> 2, p4 = lane & 3;
  const unsigned vb = (unsigned)(size_t)(lds + KT * RS) + (4 * h + q4) * RS + (16 * blk + 4 * p4) * 2;
  for (int round = 0; round < 2; ++round) {
    const int tile = round * 4 + wid;
    const bool act = tile < 6;
    const long qrow0 = (long)grp * 768 + sub * 192 + (act ? tile : 0) * 32;
    bf16x8 qf[16];
#pragma unroll
    for (int s = 0; s < 16; ++s) qf[s] = *(const bf16x8*)(Q + (qrow0 + r) * D + s * 16 + h * 8);
    f32x16 o[8];
#pragma unroll
    for (int c = 0; c < 8; ++c) o[c] = f32x16{};
    float m = -__builtin_inff(), l = 0.f;
    for (int t = 0; t < SK / KT; ++t) {
      __syncthreads();
      for (int i = tid; i < 2048; i += 256) {
        const int which = i >> 10, c = i & 1023, row = c >> 5, ch = c & 31;
        const short* src = (which ? Vg : Kg) + (long)(t * KT + row) * D + ch * 8;
        *(bf16x8*)(lds + which * KT * RS + row * RS + ch * 16) = *(const bf16x8*)src;
      }
      __syncthreads();
      f32x16 s = {};
#pragma unroll
      for (int k = 0; k < 16; ++k) {
        bf16x8 kf = *(const bf16x8*)(Ks + r * RS + k * 32 + h * 16);
        s = __builtin_amdgcn_mfma_f32_32x32x16_bf16(kf, qf[k], s, 0, 0, 0);
      }
      float mx = s[0];
#pragma unroll
      for (int i = 1; i < 16; ++i) mx = fmaxf(mx, s[i]);
      mx = fmaxf(mx, __shfl_xor(mx, 32));
      const float mn = fmaxf(m, mx);
      const float alpha = __builtin_amdgcn_exp2f((m - mn) * C);
      m = mn;
      float ps = 0.f;
#pragma unroll
      for (int i = 0; i < 16; ++i) { s[i] = __builtin_amdgcn_exp2f((s[i] - mn) * C); ps += s[i]; }
      ps += __shfl_xor(ps, 32);
      l = l * alpha + ps;
#pragma unroll
      for (int c = 0; c < 8; ++c)
#pragma unroll
        for (int i = 0; i < 16; ++i) o[c][i] *= alpha;
      bf16x8 pf[2];
#pragma unroll
      for (int ks = 0; ks < 2; ++ks) {
        u32x4 w = {cvtpk(s[8*ks], s[8*ks+1]), cvtpk(s[8*ks+2], s[8*ks+3]), cvtpk(s[8*ks+4], s[8*ks+5]), cvtpk(s[8*ks+6], s[8*ks+7])};
        pf[ks] = __builtin_bit_cast(bf16x8, w);
      }
#define PV(C_) pv1<C_,0>(o[C_], vb, pf[0]); pv1<C_,1>(o[C_], vb, pf[1]);
      PV(0) PV(1) PV(2) PV(3) PV(4) PV(5) PV(6) PV(7)
#undef PV
    }
    if (act) {
      const float il = 1.f / l;
      short* Orow = O + (qrow0 + r) * D;
#pragma unroll
      for (int c = 0; c < 8; ++c)
#pragma unroll
        for (int g = 0; g < 4; ++g) {
          u32x2 w = {cvtpk(o[c][4*g] * il, o[c][4*g+1] * il), cvtpk(o[c][4*g+2] * il, o[c][4*g+3] * il)};
          *(u32x2*)(Orow + 32 * c + 8 * g + 4 * h) = w;
        }
    }
  }
}

__global__ __launch_bounds__(256, 1)
void gqa_attn(const char* __restrict__ Q, const char* __restrict__ K, const char* __restrict__ V, char* __restrict__ O) {
  extern __shared__ __attribute__((aligned(16))) char lds[];
  const int wid = __builtin_amdgcn_readfirstlane(threadIdx.x >> 6);
  const int bid = blockIdx.x;
  const int grp = (bid & 7) * 8 + (bid >> 5), sub = (bid >> 3) & 3;
  const unsigned row0 = grp * 768 + sub * 192 + 96 * (wid >> 1);
  const unsigned qo = (row0 + 8 * (wid & 1)) * 512u, ko = grp * 262144u + wid * 4096u;
  const unsigned vo = grp * 262144u + (16 * (wid >> 1) + 2 * (wid & 1)) * 512u, oo = row0 * 512u + 64 * (wid & 1);
  const __amdgpu_buffer_rsrc_t sq = __builtin_amdgcn_make_buffer_rsrc((void*)(Q + qo), (short)0, (int)(QBYTES - qo), 0x00020000);
  const __amdgpu_buffer_rsrc_t sk = __builtin_amdgcn_make_buffer_rsrc((void*)(K + ko), (short)0, (int)(KBYTES - ko), 0x00020000);
  const __amdgpu_buffer_rsrc_t sv = __builtin_amdgcn_make_buffer_rsrc((void*)(V + vo), (short)0, (int)(KBYTES - vo), 0x00020000);
  const __amdgpu_buffer_rsrc_t so = __builtin_amdgcn_make_buffer_rsrc((void*)(O + oo), (short)0, (int)(QBYTES - oo), 0x00020000);
  unsigned flag;
  asm volatile(
    "v_mbcnt_lo_u32_b32 v242, -1, 0\n\t"
    "v_mbcnt_hi_u32_b32 v242, -1, v242\n\t"
    "s_mul_i32 s41, %5, 4224\n\t"
    "v_lshrrev_b32_e32 v243, 5, v242\n\t"
    "v_and_b32_e32 v240, 31, v242\n\t"
    "v_xor_b32_e32 v240, v240, v243\n\t"
    "v_lshlrev_b32_e32 v240, 4, v240\n\t"
    "v_lshl_add_u32 v240, v243, 9, v240\n\t"
    "s_mov_b32 s53, %7\n\t"
    "s_add_u32 s54, %7, 16384\n\t"
    "s_add_u32 m0, s41, 3328\n\t"
    "s_add_u32 s55, %7, 32768\n\t"
    "buffer_load_dwordx4 v240, %2, s53 offen offset:0 nt lds\n\t"
    "s_and_b32 s55, s55, 0x3ffff\n\t"
    "s_add_u32 s56, s55, 16384\n\t"
    "s_add_u32 m0, s41, 3360\n\t"
    "s_add_u32 s57, %7, 65536\n\t"
    "buffer_load_dwordx4 v240, %2, s53 offen offset:1024 nt lds\n\t"
    "s_and_b32 s57, s57, 0x3ffff\n\t"
    "s_add_u32 s58, s57, 16384\n\t"
    "s_add_u32 m0, s41, 3392\n\t"
    "s_add_u32 s59, %7, 98304\n\t"
    "buffer_load_dwordx4 v240, %2, s53 offen offset:2048 nt lds\n\t"
    "s_and_b32 s59, s59, 0x3ffff\n\t"
    "s_add_u32 s60, s59, 16384\n\t"
    "s_add_u32 m0, s41, 3424\n\t"
    "s_add_u32 s61, %7, 131072\n\t"
    "buffer_load_dwordx4 v240, %2, s53 offen offset:3072 nt lds\n\t"
    "s_and_b32 s61, s61, 0x3ffff\n\t"
    "s_add_u32 s62, s61, 16384\n\t"
    "s_getpc_b64 s[38:39]\n\t"
    "HERE_%=:\n\t"
    "s_add_u32 s38, s38, BODY_%=-HERE_%=\n\t"
    "s_addc_u32 s39, s39, 0\n\t"
    "s_and_b32 s47, %6, 31\n\t"
    "s_lshl_b32 s47, s47, 10\n\t"
    "v_lshl_add_u32 v230, v242, 4, s47\n\t"
    "v_min_u32_e32 v230, 29588, v230\n\t"
    "global_load_dword v228, v230, s[38:39] \n\t"
    "s_add_u32 s63, %7, 163840\n\t"
    "s_and_b32 s63, s63, 0x3ffff\n\t"
    "s_add_u32 m0, s41, 37120\n\t"
    "s_add_u32 s64, s63, 16384\n\t"
    "buffer_load_dwordx4 v240, %1, %8 offen offset:0 nt lds\n\t"
    "s_add_u32 s65, %7, 196608\n\t"
    "s_and_b32 s65, s65, 0x3ffff\n\t"
    "s_add_u32 m0, s41, 37152\n\t"
    "s_add_u32 s66, s65, 16384\n\t"
    "buffer_load_dwordx4 v240, %1, %8 offen offset:1024 nt lds\n\t"
    "s_add_u32 s67, %7, 229376\n\t"
    "s_and_b32 s67, s67, 0x3ffff\n\t"
    "s_add_u32 m0, s41, 37184\n\t"
    "s_add_u32 s68, s67, 16384\n\t"
    "buffer_load_dwordx4 v240, %1, %8 offen offset:2048 nt lds\n\t"
    "s_add_u32 s48, %8, 8192\n\t"
    "s_sub_u32 s47, s48, 49152\n\t"
    "s_add_u32 m0, s41, 37216\n\t"
    "s_min_u32 s48, s48, s47\n\t"
    "buffer_load_dwordx4 v240, %1, %8 offen offset:3072 nt lds\n\t"
    "s_add_u32 s49, %8, 16384\n\t"
    "s_sub_u32 s47, s49, 49152\n\t"
    "s_add_u32 m0, s41, 20224\n\t"
    "s_min_u32 s49, s49, s47\n\t"
    "buffer_load_dwordx4 v240, %2, s54 offen offset:0 nt lds\n\t"
    "s_add_u32 s50, %8, 24576\n\t"
    "s_sub_u32 s47, s50, 49152\n\t"
    "s_add_u32 m0, s41, 20256\n\t"
    "s_min_u32 s50, s50, s47\n\t"
    "buffer_load_dwordx4 v240, %2, s54 offen offset:1024 nt lds\n\t"
    "s_add_u32 s51, %8, 32768\n\t"
    "s_sub_u32 s47, s51, 49152\n\t"
    "s_add_u32 m0, s41, 20288\n\t"
    "s_min_u32 s51, s51, s47\n\t"
    "buffer_load_dwordx4 v240, %2, s54 offen offset:2048 nt lds\n\t"
    "s_add_u32 s52, %8, 40960\n\t"
    "s_sub_u32 s47, s52, 49152\n\t"
    "s_add_u32 m0, s41, 20320\n\t"
    "s_min_u32 s52, s52, s47\n\t"
    "buffer_load_dwordx4 v240, %2, s54 offen offset:3072 nt lds\n\t"
    "s_mov_b32 s40, 0x3db8aa3b\n\t"
    "s_mov_b32 s44, 0x57800000\n\t"
    "s_mul_i32 s42, %5, 2112\n\t"
    "s_and_b32 s45, %5, 1\n\t"
    "s_lshr_b32 s46, %5, 1\n\t"
    "v_and_b32_e32 v0, 15, v242\n\t"
    "v_lshrrev_b32_e32 v1, 4, v242\n\t"
    "v_and_b32_e32 v2, 1, v0\n\t"
    "v_xor_b32_e32 v3, v1, v2\n\t"
    "v_lshlrev_b32_e32 v3, 4, v3\n\t"
    "v_lshl_add_u32 v3, v2, 9, v3\n\t"
    "v_lshrrev_b32_e32 v4, 1, v0\n\t"
    "v_mul_u32_u24_e32 v4, 1056, v4\n\t"
    "v_add_u32_e32 v3, v3, v4\n\t"
    "s_mul_i32 s47, s45, 8448\n\t"
    "v_add_u32_e32 v238, s47, v3\n\t"
    "s_mul_i32 s47, s46, 8448\n\t"
    "v_add_u32_e32 v239, s47, v3\n\t"
    "v_bfe_u32 v4, v242, 3, 2\n\t"
    "v_lshlrev_b32_e32 v4, 11, v4\n\t"
    "v_lshl_add_u32 v4, v243, 14, v4\n\t"
    "v_and_b32_e32 v5, 7, v242\n\t"
    "v_lshl_add_u32 v250, v5, 4, v4\n\t"
    "v_bfe_u32 v4, v242, 2, 2\n\t"
    "v_mul_u32_u24_e32 v4, 1056, v4\n\t"
    "v_lshl_add_u32 v4, v1, 7, v4\n\t"
    "v_and_b32_e32 v5, 3, v242\n\t"
    "v_lshl_add_u32 v4, v5, 3, v4\n\t"
    "s_mul_i32 s47, s45, 4288\n\t"
    "v_add_u32_e32 v252, s47, v4\n\t"
    "s_mul_i32 s47, s45, 4160\n\t"
    "s_add_u32 s47, s47, 64\n\t"
    "v_add_u32_e32 v251, s47, v4\n\t"
    "s_mul_i32 s47, %5, 6144\n\t"
    "s_add_u32 s47, s47, 45568\n\t"
    "v_lshl_add_u32 v253, v242, 4, s47\n\t"
    "s_xor_b32 s47, %5, 1\n\t"
    "s_mul_i32 s47, s47, 6144\n\t"
    "s_add_u32 s47, s47, 45568\n\t"
    "v_lshl_add_u32 v254, v242, 4, s47\n\t"
    "v_and_b32_e32 v4, 1, v1\n\t"
    "v_lshlrev_b32_e32 v4, 5, v4\n\t"
    "v_lshl_add_u32 v4, v243, 4, v4\n\t"
    "v_lshl_add_u32 v255, v0, 9, v4\n\t"
    "s_mul_i32 s47, s46, 384\n\t"
    "s_add_u32 s47, s47, 0\n\t"
    "v_lshl_add_u32 v241, v0, 2, s47\n\t"
    "s_and_b32 s37, %6, 3\n\t"
    "s_lshl_b32 s37, s37, 16\n\t"
    "s_mul_i32 s47, s46, 24576\n\t"
    "s_add_u32 s37, s37, s47\n\t"
    "s_mul_i32 s47, s45, 15360\n\t"
    "s_add_u32 s37, s37, s47\n\t"
    "v_lshrrev_b32_e32 v4, 2, v242\n\t"
    "v_and_b32_e32 v5, 3, v242\n\t"
    "v_add_u32_e32 v5, 4, v5\n\t"
    "v_lshlrev_b32_e32 v5, 6, v5\n\t"
    "v_lshl_add_u32 v237, v4, 9, v5\n\t"
    "v_mov_b32_e32 v232, 0x3f803f80\n\t"
    "v_mov_b32_e32 v233, 0x3f803f80\n\t"
    "v_mov_b32_e32 v234, 0x3f803f80\n\t"
    "v_mov_b32_e32 v235, 0x3f803f80\n\t"
    "v_mov_b32_e32 v6, 0xff800000\n\t"
    "ds_write_b32 v241, v6 offset:0\n\t"
    "ds_write_b32 v241, v6 offset:64\n\t"
    "ds_write_b32 v241, v6 offset:128\n\t"
    "ds_write_b32 v241, v6 offset:192\n\t"
    "ds_write_b32 v241, v6 offset:256\n\t"
    "ds_write_b32 v241, v6 offset:320\n\t"
    "s_branch BODY_%=\n\t"
    ".p2align 12\n\t"
    "BODY_%=:\n\t"
    "s_add_u32 m0, s41, 70912\n\t"
    "s_waitcnt vmcnt(7)\n\t"
    "buffer_load_dwordx4 v240, %1, s48 offen offset:0 nt lds\n\t"
    "s_add_u32 m0, s41, 70944\n\t"
    "s_waitcnt vmcnt(7)\n\t"
    "buffer_load_dwordx4 v240, %1, s48 offen offset:1024 nt lds\n\t"
    "s_add_u32 m0, s41, 70976\n\t"
    "s_waitcnt vmcnt(7)\n\t"
    "buffer_load_dwordx4 v240, %1, s48 offen offset:2048 nt lds\n\t"
    "s_add_u32 m0, s41, 71008\n\t"
    "s_waitcnt vmcnt(7)\n\t"
    "buffer_load_dwordx4 v240, %1, s48 offen offset:3072 nt lds\n\t"
    "s_mov_b64 exec, 0\n\t"
    "s_cmp_eq_u32 %5, 0\n\t"
    "s_cbranch_scc1 PG1_%=\n\t"
    "s_cmp_eq_u32 %5, 1\n\t"
    "s_cbranch_scc1 PG2_%=\n\t"
    "s_cmp_eq_u32 %5, 2\n\t"
    "s_cbranch_scc1 PG3_%=\n\t"
    "s_branch PG4_%=\n\t"
    "RET_%=:\n\t"
    "s_mov_b64 exec, -1\n\t"
    "s_add_u32 m0, s41, 54016\n\t"
    "s_waitcnt vmcnt(7)\n\t"
    "buffer_load_dwordx4 v240, %2, s55 offen offset:0 nt lds\n\t"
    "s_add_u32 m0, s41, 54048\n\t"
    "s_waitcnt vmcnt(7)\n\t"
    "buffer_load_dwordx4 v240, %2, s55 offen offset:1024 nt lds\n\t"
    "s_add_u32 m0, s41, 54080\n\t"
    "s_waitcnt vmcnt(7)\n\t"
    "buffer_load_dwordx4 v240, %2, s55 offen offset:2048 nt lds\n\t"
    "s_add_u32 m0, s41, 54112\n\t"
    "s_waitcnt vmcnt(7)\n\t"
    "buffer_load_dwordx4 v240, %2, s55 offen offset:3072 nt lds\n\t"
    "s_waitcnt lgkmcnt(5)\n\t"
    "s_barrier\n\t"
    "ds_read_b128 v[96:99], v238 offset:3328\n\t"
    "ds_read_b128 a[224:227], v239 offset:37120\n\t"
    "ds_read_b128 v[100:103], v238 offset:3392\n\t"
    "ds_read_b128 a[228:231], v239 offset:37184\n\t"
    "ds_read_b128 v[104:107], v238 offset:3456\n\t"
    "ds_read_b128 a[232:235], v239 offset:37248\n\t"
    "ds_read_b128 v[108:111], v238 offset:3520\n\t"
    "ds_read_b128 a[236:239], v239 offset:37312\n\t"
    "ds_read_b128 v[112:115], v238 offset:3584\n\t"
    "ds_read_b128 a[240:243], v239 offset:37376\n\t"
    "ds_read_b128 v[116:119], v238 offset:3648\n\t"
    "ds_read_b128 a[244:247], v239 offset:37440\n\t"
    "ds_read_b128 v[120:123], v238 offset:3712\n\t"
    "ds_read_b128 a[248:251], v239 offset:37504\n\t"
    "ds_read_b128 v[124:127], v238 offset:3776\n\t"
    "ds_read_b128 a[252:255], v239 offset:37568\n\t"
    "ds_read_b128 v[128:131], v238 offset:20224\n\t"
    "ds_read_b128 v[132:135], v238 offset:20288\n\t"
    "ds_read_b128 v[136:139], v238 offset:20352\n\t"
    "ds_read_b128 v[140:143], v238 offset:20416\n\t"
    "ds_read_b128 v[144:147], v238 offset:20480\n\t"
    "ds_read_b128 v[148:151], v238 offset:20544\n\t"
    "ds_read_b128 v[152:155], v238 offset:20608\n\t"
    "ds_read_b128 v[156:159], v238 offset:20672\n\t"
    "s_waitcnt lgkmcnt(14)\n\t"
    "v_mfma_f32_16x16x32_bf16 v[4:7], v[96:99], a[224:227], 0\n\t"
    "v_mfma_f32_16x16x32_bf16 v[4:7], v[100:103], a[228:231], v[4:7]\n\t"
    "v_mfma_f32_16x16x32_bf16 v[4:7], v[104:107], a[232:235], v[4:7]\n\t"
    "v_mfma_f32_16x16x32_bf16 v[4:7], v[108:111], a[236:239], v[4:7]\n\t"
    "v_mfma_f32_16x16x32_bf16 v[4:7], v[112:115], a[240:243], v[4:7]\n\t"
    "s_waitcnt lgkmcnt(12)\n\t"
    "v_mfma_f32_16x16x32_bf16 v[4:7], v[116:119], a[244:247], v[4:7]\n\t"
    "s_waitcnt lgkmcnt(10)\n\t"
    "v_mfma_f32_16x16x32_bf16 v[4:7], v[120:123], a[248:251], v[4:7]\n\t"
    "s_waitcnt lgkmcnt(8)\n\t"
    "v_mfma_f32_16x16x32_bf16 v[4:7], v[124:127], a[252:255], v[4:7]\n\t"
    "s_waitcnt lgkmcnt(7)\n\t"
    "v_mfma_f32_16x16x32_bf16 v[24:27], v[128:131], a[224:227], 0\n\t"
    "s_waitcnt lgkmcnt(6)\n\t"
    "v_mfma_f32_16x16x32_bf16 v[24:27], v[132:135], a[228:231], v[24:27]\n\t"
    "s_waitcnt lgkmcnt(5)\n\t"
    "v_mfma_f32_16x16x32_bf16 v[24:27], v[136:139], a[232:235], v[24:27]\n\t"
    "s_waitcnt lgkmcnt(4)\n\t"
    "v_mfma_f32_16x16x32_bf16 v[24:27], v[140:143], a[236:239], v[24:27]\n\t"
    "s_waitcnt lgkmcnt(3)\n\t"
    "v_mfma_f32_16x16x32_bf16 v[24:27], v[144:147], a[240:243], v[24:27]\n\t"
    "s_waitcnt lgkmcnt(2)\n\t"
    "v_mfma_f32_16x16x32_bf16 v[24:27], v[148:151], a[244:247], v[24:27]\n\t"
    "s_waitcnt lgkmcnt(1)\n\t"
    "v_mfma_f32_16x16x32_bf16 v[24:27], v[152:155], a[248:251], v[24:27]\n\t"
    "s_waitcnt lgkmcnt(0)\n\t"
    "v_mfma_f32_16x16x32_bf16 v[24:27], v[156:159], a[252:255], v[24:27]\n\t"
    "v_max3_f32 v244, v4, v5, v6\n\t"
    "s_nop 6\n\t"
    "v_max3_f32 v244, v244, v7, v24\n\t"
    "v_max3_f32 v244, v244, v25, v26\n\t"
    "v_max_f32_e32 v244, v244, v27\n\t"
    "ds_max_f32 v241, v244 offset:0\n\t"
    "s_add_u32 m0, s41, 104704\n\t"
    "s_waitcnt vmcnt(7)\n\t"
    "buffer_load_dwordx4 v240, %1, s49 offen offset:0 nt lds\n\t"
    "s_add_u32 m0, s41, 104736\n\t"
    "s_waitcnt vmcnt(7)\n\t"
    "buffer_load_dwordx4 v240, %1, s49 offen offset:1024 nt lds\n\t"
    "s_add_u32 m0, s41, 104768\n\t"
    "s_waitcnt vmcnt(7)\n\t"
    "buffer_load_dwordx4 v240, %1, s49 offen offset:2048 nt lds\n\t"
    "s_add_u32 m0, s41, 104800\n\t"
    "s_waitcnt vmcnt(7)\n\t"
    "buffer_load_dwordx4 v240, %1, s49 offen offset:3072 nt lds\n\t"
    "s_waitcnt lgkmcnt(0)\n\t"
    "s_barrier\n\t"
    "ds_read_b32 v244, v241 offset:0\n\t"
    "v_add_u32_e32 v239, 0xc600, v239\n\t"
    "ds_read_b128 a[64:67], v239 offset:20224\n\t"
    "ds_read_b128 a[68:71], v239 offset:20288\n\t"
    "ds_read_b128 a[72:75], v239 offset:20352\n\t"
    "ds_read_b128 a[76:79], v239 offset:20416\n\t"
    "ds_read_b128 a[80:83], v239 offset:20480\n\t"
    "ds_read_b128 a[84:87], v239 offset:20544\n\t"
    "ds_read_b128 a[88:91], v239 offset:20608\n\t"
    "ds_read_b128 a[92:95], v239 offset:20672\n\t"
    "s_waitcnt lgkmcnt(8)\n\t"
    "v_mul_f32_e32 v244, 0xbdb8aa3b, v244\n\t"
    "v_fma_f32 v4, v4, s40, v244\n\t"
    "v_fma_f32 v5, v5, s40, v244\n\t"
    "v_fma_f32 v24, v24, s40, v244\n\t"
    "v_fma_f32 v6, v6, s40, v244\n\t"
    "v_fma_f32 v25, v25, s40, v244\n\t"
    "v_fma_f32 v7, v7, s40, v244\n\t"
    "v_fma_f32 v26, v26, s40, v244\n\t"
    "v_fma_f32 v27, v27, s40, v244\n\t"
    "v_exp_f32_e32 v4, v4\n\t"
    "v_exp_f32_e32 v24, v24\n\t"
    "v_exp_f32_e32 v5, v5\n\t"
    "v_exp_f32_e32 v25, v25\n\t"
    "v_exp_f32_e32 v6, v6\n\t"
    "v_exp_f32_e32 v26, v26\n\t"
    "v_exp_f32_e32 v7, v7\n\t"
    "v_exp_f32_e32 v27, v27\n\t"
    "v_cvt_pk_bf16_f32 v2, v24, v25\n\t"
    "v_cvt_pk_bf16_f32 v0, v4, v5\n\t"
    "v_cvt_pk_bf16_f32 v3, v26, v27\n\t"
    "v_cvt_pk_bf16_f32 v1, v6, v7\n\t"
    "s_waitcnt lgkmcnt(7)\n\t"
    "v_mfma_f32_16x16x32_bf16 v[28:31], v[96:99], a[64:67], 0\n\t"
    "s_waitcnt lgkmcnt(6)\n\t"
    "v_mfma_f32_16x16x32_bf16 v[28:31], v[100:103], a[68:71], v[28:31]\n\t"
    "s_waitcnt lgkmcnt(5)\n\t"
    "v_mfma_f32_16x16x32_bf16 v[28:31], v[104:107], a[72:75], v[28:31]\n\t"
    "s_waitcnt lgkmcnt(4)\n\t"
    "v_mfma_f32_16x16x32_bf16 v[28:31], v[108:111], a[76:79], v[28:31]\n\t"
    "s_waitcnt lgkmcnt(3)\n\t"
    "v_mfma_f32_16x16x32_bf16 v[28:31], v[112:115], a[80:83], v[28:31]\n\t"
    "s_waitcnt lgkmcnt(2)\n\t"
    "v_mfma_f32_16x16x32_bf16 v[28:31], v[116:119], a[84:87], v[28:31]\n\t"
    "s_waitcnt lgkmcnt(1)\n\t"
    "v_mfma_f32_16x16x32_bf16 v[28:31], v[120:123], a[88:91], v[28:31]\n\t"
    "s_waitcnt lgkmcnt(0)\n\t"
    "v_mfma_f32_16x16x32_bf16 v[28:31], v[124:127], a[92:95], v[28:31]\n\t"
    "v_mfma_f32_16x16x32_bf16 v[208:211], v[232:235], v[0:3], 0\n\t"
    "v_mfma_f32_16x16x32_bf16 v[24:27], v[128:131], a[64:67], 0\n\t"
    "v_mfma_f32_16x16x32_bf16 v[24:27], v[132:135], a[68:71], v[24:27]\n\t"
    "v_mfma_f32_16x16x32_bf16 v[24:27], v[136:139], a[72:75], v[24:27]\n\t"
    "v_mfma_f32_16x16x32_bf16 v[24:27], v[140:143], a[76:79], v[24:27]\n\t"
    "v_mfma_f32_16x16x32_bf16 v[24:27], v[144:147], a[80:83], v[24:27]\n\t"
    "v_mfma_f32_16x16x32_bf16 v[24:27], v[148:151], a[84:87], v[24:27]\n\t"
    "v_mfma_f32_16x16x32_bf16 v[24:27], v[152:155], a[88:91], v[24:27]\n\t"
    "v_mfma_f32_16x16x32_bf16 v[24:27], v[156:159], a[92:95], v[24:27]\n\t"
    "v_max3_f32 v245, v28, v29, v30\n\t"
    "s_nop 6\n\t"
    "v_max3_f32 v245, v245, v31, v24\n\t"
    "v_max3_f32 v245, v245, v25, v26\n\t"
    "v_max_f32_e32 v245, v245, v27\n\t"
    "ds_max_f32 v241, v245 offset:64\n\t"
    "s_add_u32 m0, s41, 87808\n\t"
    "s_waitcnt vmcnt(7)\n\t"
    "buffer_load_dwordx4 v240, %2, s56 offen offset:0 nt lds\n\t"
    "s_add_u32 m0, s41, 87840\n\t"
    "s_waitcnt vmcnt(7)\n\t"
    "buffer_load_dwordx4 v240, %2, s56 offen offset:1024 nt lds\n\t"
    "s_add_u32 m0, s41, 87872\n\t"
    "s_waitcnt vmcnt(7)\n\t"
    "buffer_load_dwordx4 v240, %2, s56 offen offset:2048 nt lds\n\t"
    "s_add_u32 m0, s41, 87904\n\t"
    "s_waitcnt vmcnt(7)\n\t"
    "buffer_load_dwordx4 v240, %2, s56 offen offset:3072 nt lds\n\t"
    "s_waitcnt lgkmcnt(0)\n\t"
    "s_barrier\n\t"
    "ds_read_b32 v245, v241 offset:64\n\t"
    "v_add_u32_e32 v238, 0xc600, v238\n\t"
    "ds_read_b128 v[160:163], v238 offset:3328\n\t"
    "ds_read_b128 v[164:167], v238 offset:3392\n\t"
    "ds_read_b128 v[168:171], v238 offset:3456\n\t"
    "ds_read_b128 v[172:175], v238 offset:3520\n\t"
    "ds_read_b128 v[176:179], v238 offset:3584\n\t"
    "ds_read_b128 v[180:183], v238 offset:3648\n\t"
    "ds_read_b128 v[184:187], v238 offset:3712\n\t"
    "ds_read_b128 v[188:191], v238 offset:3776\n\t"
    "s_waitcnt lgkmcnt(8)\n\t"
    "v_mul_f32_e32 v245, 0xbdb8aa3b, v245\n\t"
    "v_fma_f32 v28, v28, s40, v245\n\t"
    "v_fma_f32 v29, v29, s40, v245\n\t"
    "v_fma_f32 v24, v24, s40, v245\n\t"
    "v_fma_f32 v30, v30, s40, v245\n\t"
    "v_fma_f32 v25, v25, s40, v245\n\t"
    "v_fma_f32 v31, v31, s40, v245\n\t"
    "v_fma_f32 v26, v26, s40, v245\n\t"
    "v_fma_f32 v27, v27, s40, v245\n\t"
    "v_exp_f32_e32 v28, v28\n\t"
    "v_exp_f32_e32 v24, v24\n\t"
    "v_exp_f32_e32 v29, v29\n\t"
    "v_exp_f32_e32 v25, v25\n\t"
    "v_exp_f32_e32 v30, v30\n\t"
    "v_exp_f32_e32 v26, v26\n\t"
    "v_exp_f32_e32 v31, v31\n\t"
    "v_exp_f32_e32 v27, v27\n\t"
    "v_cvt_pk_bf16_f32 v6, v24, v25\n\t"
    "v_cvt_pk_bf16_f32 v4, v28, v29\n\t"
    "v_cvt_pk_bf16_f32 v7, v26, v27\n\t"
    "v_cvt_pk_bf16_f32 v5, v30, v31\n\t"
    "s_waitcnt lgkmcnt(7)\n\t"
    "v_mfma_f32_16x16x32_bf16 v[8:11], v[160:163], a[224:227], 0\n\t"
    "s_waitcnt lgkmcnt(6)\n\t"
    "v_mfma_f32_16x16x32_bf16 v[8:11], v[164:167], a[228:231], v[8:11]\n\t"
    "s_waitcnt lgkmcnt(5)\n\t"
    "v_mfma_f32_16x16x32_bf16 v[8:11], v[168:171], a[232:235], v[8:11]\n\t"
    "s_waitcnt lgkmcnt(4)\n\t"
    "v_mfma_f32_16x16x32_bf16 v[8:11], v[172:175], a[236:239], v[8:11]\n\t"
    "s_waitcnt lgkmcnt(3)\n\t"
    "v_mfma_f32_16x16x32_bf16 v[8:11], v[176:179], a[240:243], v[8:11]\n\t"
    "s_waitcnt lgkmcnt(2)\n\t"
    "v_mfma_f32_16x16x32_bf16 v[8:11], v[180:183], a[244:247], v[8:11]\n\t"
    "s_waitcnt lgkmcnt(1)\n\t"
    "v_mfma_f32_16x16x32_bf16 v[8:11], v[184:187], a[248:251], v[8:11]\n\t"
    "s_waitcnt lgkmcnt(0)\n\t"
    "v_mfma_f32_16x16x32_bf16 v[8:11], v[188:191], a[252:255], v[8:11]\n\t"
    "s_add_u32 m0, s41, 138496\n\t"
    "s_waitcnt vmcnt(7)\n\t"
    "buffer_load_dwordx4 v240, %1, s50 offen offset:0 nt lds\n\t"
    "s_add_u32 m0, s41, 138528\n\t"
    "s_waitcnt vmcnt(7)\n\t"
    "buffer_load_dwordx4 v240, %1, s50 offen offset:1024 nt lds\n\t"
    "s_add_u32 m0, s41, 138560\n\t"
    "s_waitcnt vmcnt(7)\n\t"
    "buffer_load_dwordx4 v240, %1, s50 offen offset:2048 nt lds\n\t"
    "s_add_u32 m0, s41, 138592\n\t"
    "s_waitcnt vmcnt(7)\n\t"
    "buffer_load_dwordx4 v240, %1, s50 offen offset:3072 nt lds\n\t"
    "s_barrier\n\t"
    "v_add_u32_e32 v239, 0xc600, v239\n\t"
    "v_mfma_f32_16x16x32_bf16 v[212:215], v[232:235], v[4:7], 0\n\t"
    "ds_read_b128 a[96:99], v239 offset:3328\n\t"
    "v_fma_f32 v8, v8, s40, v244\n\t"
    "v_mfma_f32_16x16x32_bf16 v[32:35], v[160:163], a[64:67], 0\n\t"
    "ds_read_b128 a[100:103], v239 offset:3392\n\t"
    "v_fma_f32 v9, v9, s40, v244\n\t"
    "v_mfma_f32_16x16x32_bf16 v[32:35], v[164:167], a[68:71], v[32:35]\n\t"
    "ds_read_b128 a[104:107], v239 offset:3456\n\t"
    "v_fma_f32 v10, v10, s40, v244\n\t"
    "v_fma_f32 v11, v11, s40, v244\n\t"
    "v_mfma_f32_16x16x32_bf16 v[32:35], v[168:171], a[72:75], v[32:35]\n\t"
    "ds_read_b128 a[108:111], v239 offset:3520\n\t"
    "v_exp_f32_e32 v8, v8\n\t"
    "v_mfma_f32_16x16x32_bf16 v[32:35], v[172:175], a[76:79], v[32:35]\n\t"
    "ds_read_b128 a[112:115], v239 offset:3584\n\t"
    "v_exp_f32_e32 v9, v9\n\t"
    "v_mfma_f32_16x16x32_bf16 v[32:35], v[176:179], a[80:83], v[32:35]\n\t"
    "ds_read_b128 a[116:119], v239 offset:3648\n\t"
    "v_exp_f32_e32 v10, v10\n\t"
    "v_mfma_f32_16x16x32_bf16 v[32:35], v[180:183], a[84:87], v[32:35]\n\t"
    "ds_read_b128 a[120:123], v239 offset:3712\n\t"
    "ds_read_b128 a[124:127], v239 offset:3776\n\t"
    "v_mfma_f32_16x16x32_bf16 v[32:35], v[184:187], a[88:91], v[32:35]\n\t"
    "v_exp_f32_e32 v11, v11\n\t"
    "v_cvt_pk_bf16_f32 v24, v8, v9\n\t"
    "v_mfma_f32_16x16x32_bf16 v[32:35], v[188:191], a[92:95], v[32:35]\n\t"
    "s_waitcnt lgkmcnt(7)\n\t"
    "v_cvt_pk_bf16_f32 v25, v10, v11\n\t"
    "v_mfma_f32_16x16x32_bf16 v[12:15], v[96:99], a[96:99], 0\n\t"
    "s_waitcnt lgkmcnt(6)\n\t"
    "v_mfma_f32_16x16x32_bf16 v[12:15], v[100:103], a[100:103], v[12:15]\n\t"
    "s_waitcnt lgkmcnt(5)\n\t"
    "v_mfma_f32_16x16x32_bf16 v[12:15], v[104:107], a[104:107], v[12:15]\n\t"
    "s_waitcnt lgkmcnt(4)\n\t"
    "v_mfma_f32_16x16x32_bf16 v[12:15], v[108:111], a[108:111], v[12:15]\n\t"
    "s_waitcnt lgkmcnt(3)\n\t"
    "v_fma_f32 v32, v32, s40, v245\n\t"
    "v_fma_f32 v33, v33, s40, v245\n\t"
    "v_mfma_f32_16x16x32_bf16 v[12:15], v[112:115], a[112:115], v[12:15]\n\t"
    "s_waitcnt lgkmcnt(2)\n\t"
    "v_fma_f32 v34, v34, s40, v245\n\t"
    "v_fma_f32 v35, v35, s40, v245\n\t"
    "v_mfma_f32_16x16x32_bf16 v[12:15], v[116:119], a[116:119], v[12:15]\n\t"
    "s_waitcnt lgkmcnt(1)\n\t"
    "v_exp_f32_e32 v32, v32\n\t"
    "v_mfma_f32_16x16x32_bf16 v[12:15], v[120:123], a[120:123], v[12:15]\n\t"
    "s_waitcnt lgkmcnt(0)\n\t"
    "v_exp_f32_e32 v33, v33\n\t"
    "v_mfma_f32_16x16x32_bf16 v[12:15], v[124:127], a[124:127], v[12:15]\n\t"
    "v_exp_f32_e32 v34, v34\n\t"
    "v_mfma_f32_16x16x32_bf16 v[36:39], v[128:131], a[96:99], 0\n\t"
    "v_exp_f32_e32 v35, v35\n\t"
    "v_mfma_f32_16x16x32_bf16 v[36:39], v[132:135], a[100:103], v[36:39]\n\t"
    "v_cvt_pk_bf16_f32 v28, v32, v33\n\t"
    "v_cvt_pk_bf16_f32 v29, v34, v35\n\t"
    "v_mfma_f32_16x16x32_bf16 v[36:39], v[136:139], a[104:107], v[36:39]\n\t"
    "v_mfma_f32_16x16x32_bf16 v[36:39], v[140:143], a[108:111], v[36:39]\n\t"
    "v_mfma_f32_16x16x32_bf16 v[36:39], v[144:147], a[112:115], v[36:39]\n\t"
    "v_mfma_f32_16x16x32_bf16 v[36:39], v[148:151], a[116:119], v[36:39]\n\t"
    "v_mfma_f32_16x16x32_bf16 v[36:39], v[152:155], a[120:123], v[36:39]\n\t"
    "v_mfma_f32_16x16x32_bf16 v[36:39], v[156:159], a[124:127], v[36:39]\n\t"
    "v_mfma_f32_16x16x32_bf16 v[48:51], v[160:163], a[96:99], 0\n\t"
    "v_mfma_f32_16x16x32_bf16 v[48:51], v[164:167], a[100:103], v[48:51]\n\t"
    "v_mfma_f32_16x16x32_bf16 v[48:51], v[168:171], a[104:107], v[48:51]\n\t"
    "v_max3_f32 v246, v12, v13, v14\n\t"
    "s_nop 3\n\t"
    "v_max3_f32 v246, v246, v15, v36\n\t"
    "v_mfma_f32_16x16x32_bf16 v[48:51], v[172:175], a[108:111], v[48:51]\n\t"
    "v_max3_f32 v246, v246, v37, v38\n\t"
    "v_max_f32_e32 v246, v246, v39\n\t"
    "v_mfma_f32_16x16x32_bf16 v[48:51], v[176:179], a[112:115], v[48:51]\n\t"
    "ds_max_f32 v241, v246 offset:128\n\t"
    "v_mfma_f32_16x16x32_bf16 v[48:51], v[180:183], a[116:119], v[48:51]\n\t"
    "v_mfma_f32_16x16x32_bf16 v[48:51], v[184:187], a[120:123], v[48:51]\n\t"
    "v_mfma_f32_16x16x32_bf16 v[48:51], v[188:191], a[124:127], v[48:51]\n\t"
    "s_add_u32 m0, s41, 121600\n\t"
    "s_waitcnt vmcnt(7)\n\t"
    "buffer_load_dwordx4 v240, %2, s57 offen offset:0 nt lds\n\t"
    "s_add_u32 m0, s41, 121632\n\t"
    "s_waitcnt vmcnt(7)\n\t"
    "buffer_load_dwordx4 v240, %2, s57 offen offset:1024 nt lds\n\t"
    "s_add_u32 m0, s41, 121664\n\t"
    "s_waitcnt vmcnt(7)\n\t"
    "buffer_load_dwordx4 v240, %2, s57 offen offset:2048 nt lds\n\t"
    "s_add_u32 m0, s41, 121696\n\t"
    "s_waitcnt vmcnt(7)\n\t"
    "buffer_load_dwordx4 v240, %2, s57 offen offset:3072 nt lds\n\t"
    "s_waitcnt lgkmcnt(0)\n\t"
    "s_barrier\n\t"
    "ds_read_b32 v246, v241 offset:128\n\t"
    "ds_read_b128 a[0:3], v238 offset:37120\n\t"
    "ds_read_b128 a[4:7], v238 offset:37184\n\t"
    "ds_read_b128 a[8:11], v238 offset:37248\n\t"
    "ds_read_b128 a[12:15], v238 offset:37312\n\t"
    "ds_read_b128 a[16:19], v238 offset:37376\n\t"
    "ds_read_b128 a[20:23], v238 offset:37440\n\t"
    "ds_read_b128 a[24:27], v238 offset:37504\n\t"
    "ds_read_b128 a[28:31], v238 offset:37568\n\t"
    "s_waitcnt lgkmcnt(8)\n\t"
    "v_mul_f32_e32 v246, 0xbdb8aa3b, v246\n\t"
    "v_fma_f32 v12, v12, s40, v246\n\t"
    "v_fma_f32 v13, v13, s40, v246\n\t"
    "v_fma_f32 v36, v36, s40, v246\n\t"
    "v_fma_f32 v14, v14, s40, v246\n\t"
    "v_fma_f32 v37, v37, s40, v246\n\t"
    "v_fma_f32 v15, v15, s40, v246\n\t"
    "v_fma_f32 v38, v38, s40, v246\n\t"
    "v_fma_f32 v39, v39, s40, v246\n\t"
    "v_exp_f32_e32 v12, v12\n\t"
    "v_exp_f32_e32 v36, v36\n\t"
    "v_exp_f32_e32 v13, v13\n\t"
    "v_exp_f32_e32 v37, v37\n\t"
    "v_exp_f32_e32 v14, v14\n\t"
    "v_exp_f32_e32 v38, v38\n\t"
    "v_exp_f32_e32 v15, v15\n\t"
    "v_exp_f32_e32 v39, v39\n\t"
    "v_cvt_pk_bf16_f32 v10, v36, v37\n\t"
    "v_cvt_pk_bf16_f32 v8, v12, v13\n\t"
    "v_cvt_pk_bf16_f32 v11, v38, v39\n\t"
    "v_fma_f32 v48, v48, s40, v246\n\t"
    "v_cvt_pk_bf16_f32 v9, v14, v15\n\t"
    "v_fma_f32 v49, v49, s40, v246\n\t"
    "s_waitcnt lgkmcnt(7)\n\t"
    "v_mfma_f32_16x16x32_bf16 v[52:55], a[0:3], a[224:227], 0\n\t"
    "s_waitcnt lgkmcnt(6)\n\t"
    "v_fma_f32 v50, v50, s40, v246\n\t"
    "v_fma_f32 v51, v51, s40, v246\n\t"
    "v_mfma_f32_16x16x32_bf16 v[52:55], a[4:7], a[228:231], v[52:55]\n\t"
    "s_waitcnt lgkmcnt(5)\n\t"
    "v_exp_f32_e32 v48, v48\n\t"
    "v_mfma_f32_16x16x32_bf16 v[52:55], a[8:11], a[232:235], v[52:55]\n\t"
    "s_waitcnt lgkmcnt(4)\n\t"
    "v_exp_f32_e32 v49, v49\n\t"
    "v_mfma_f32_16x16x32_bf16 v[52:55], a[12:15], a[236:239], v[52:55]\n\t"
    "s_waitcnt lgkmcnt(0)\n\t"
    "v_exp_f32_e32 v50, v50\n\t"
    "v_mfma_f32_16x16x32_bf16 v[52:55], a[16:19], a[240:243], v[52:55]\n\t"
    "v_exp_f32_e32 v51, v51\n\t"
    "v_mfma_f32_16x16x32_bf16 v[52:55], a[20:23], a[244:247], v[52:55]\n\t"
    "v_cvt_pk_bf16_f32 v32, v48, v49\n\t"
    "v_cvt_pk_bf16_f32 v33, v50, v51\n\t"
    "v_mfma_f32_16x16x32_bf16 v[52:55], a[24:27], a[248:251], v[52:55]\n\t"
    "v_mfma_f32_16x16x32_bf16 v[52:55], a[28:31], a[252:255], v[52:55]\n\t"
    "v_mfma_f32_16x16x32_bf16 v[216:219], v[232:235], v[8:11], 0\n\t"
    "v_mfma_f32_16x16x32_bf16 v[12:15], a[0:3], a[64:67], 0\n\t"
    "v_mfma_f32_16x16x32_bf16 v[12:15], a[4:7], a[68:71], v[12:15]\n\t"
    "v_mfma_f32_16x16x32_bf16 v[12:15], a[8:11], a[72:75], v[12:15]\n\t"
    "s_nop 3\n\t"
    "v_fma_f32 v52, v52, s40, v244\n\t"
    "v_fma_f32 v53, v53, s40, v244\n\t"
    "v_mfma_f32_16x16x32_bf16 v[12:15], a[12:15], a[76:79], v[12:15]\n\t"
    "v_fma_f32 v54, v54, s40, v244\n\t"
    "v_fma_f32 v55, v55, s40, v244\n\t"
    "v_mfma_f32_16x16x32_bf16 v[12:15], a[16:19], a[80:83], v[12:15]\n\t"
    "v_exp_f32_e32 v52, v52\n\t"
    "v_mfma_f32_16x16x32_bf16 v[12:15], a[20:23], a[84:87], v[12:15]\n\t"
    "v_exp_f32_e32 v53, v53\n\t"
    "v_mfma_f32_16x16x32_bf16 v[12:15], a[24:27], a[88:91], v[12:15]\n\t"
    "v_exp_f32_e32 v54, v54\n\t"
    "v_mfma_f32_16x16x32_bf16 v[12:15], a[28:31], a[92:95], v[12:15]\n\t"
    "s_add_u32 m0, s41, 20224\n\t"
    "s_waitcnt vmcnt(7)\n\t"
    "buffer_load_dwordx4 v240, %1, s51 offen offset:0 nt lds\n\t"
    "s_add_u32 m0, s41, 20256\n\t"
    "s_waitcnt vmcnt(7)\n\t"
    "buffer_load_dwordx4 v240, %1, s51 offen offset:1024 nt lds\n\t"
    "s_add_u32 m0, s41, 20288\n\t"
    "s_waitcnt vmcnt(7)\n\t"
    "buffer_load_dwordx4 v240, %1, s51 offen offset:2048 nt lds\n\t"
    "s_add_u32 m0, s41, 20320\n\t"
    "s_waitcnt vmcnt(7)\n\t"
    "buffer_load_dwordx4 v240, %1, s51 offen offset:3072 nt lds\n\t"
    "s_barrier\n\t"
    "ds_read_b128 a[128:131], v239 offset:37120\n\t"
    "v_exp_f32_e32 v55, v55\n\t"
    "v_cvt_pk_bf16_f32 v26, v52, v53\n\t"
    "v_mfma_f32_16x16x32_bf16 v[36:39], a[0:3], a[96:99], 0\n\t"
    "ds_read_b128 a[132:135], v239 offset:37184\n\t"
    "v_cvt_pk_bf16_f32 v27, v54, v55\n\t"
    "v_fma_f32 v12, v12, s40, v245\n\t"
    "v_mfma_f32_16x16x32_bf16 v[36:39], a[4:7], a[100:103], v[36:39]\n\t"
    "ds_read_b128 a[136:139], v239 offset:37248\n\t"
    "v_fma_f32 v13, v13, s40, v245\n\t"
    "v_mfma_f32_16x16x32_bf16 v[36:39], a[8:11], a[104:107], v[36:39]\n\t"
    "ds_read_b128 a[140:143], v239 offset:37312\n\t"
    "v_fma_f32 v14, v14, s40, v245\n\t"
    "v_fma_f32 v15, v15, s40, v245\n\t"
    "v_mfma_f32_16x16x32_bf16 v[36:39], a[12:15], a[108:111], v[36:39]\n\t"
    "ds_read_b128 a[144:147], v239 offset:37376\n\t"
    "v_exp_f32_e32 v12, v12\n\t"
    "v_mfma_f32_16x16x32_bf16 v[36:39], a[16:19], a[112:115], v[36:39]\n\t"
    "ds_read_b128 a[148:151], v239 offset:37440\n\t"
    "v_exp_f32_e32 v13, v13\n\t"
    "v_mfma_f32_16x16x32_bf16 v[36:39], a[20:23], a[116:119], v[36:39]\n\t"
    "ds_read_b128 a[152:155], v239 offset:37504\n\t"
    "ds_read_b128 a[156:159], v239 offset:37568\n\t"
    "v_mfma_f32_16x16x32_bf16 v[36:39], a[24:27], a[120:123], v[36:39]\n\t"
    "v_exp_f32_e32 v14, v14\n\t"
    "v_mfma_f32_16x16x32_bf16 v[36:39], a[28:31], a[124:127], v[36:39]\n\t"
    "v_exp_f32_e32 v15, v15\n\t"
    "v_cvt_pk_bf16_f32 v30, v12, v13\n\t"
    "v_mfma_f32_16x16x32_bf16 v[208:211], v[232:235], v[24:27], v[208:211]\n\t"
    "s_waitcnt lgkmcnt(7)\n\t"
    "v_cvt_pk_bf16_f32 v31, v14, v15\n\t"
    "v_mfma_f32_16x16x32_bf16 v[48:51], v[96:99], a[128:131], 0\n\t"
    "s_waitcnt lgkmcnt(6)\n\t"
    "v_mfma_f32_16x16x32_bf16 v[48:51], v[100:103], a[132:135], v[48:51]\n\t"
    "s_waitcnt lgkmcnt(5)\n\t"
    "v_mfma_f32_16x16x32_bf16 v[48:51], v[104:107], a[136:139], v[48:51]\n\t"
    "s_waitcnt lgkmcnt(4)\n\t"
    "v_fma_f32 v36, v36, s40, v246\n\t"
    "v_fma_f32 v37, v37, s40, v246\n\t"
    "v_mfma_f32_16x16x32_bf16 v[48:51], v[108:111], a[140:143], v[48:51]\n\t"
    "s_waitcnt lgkmcnt(3)\n\t"
    "v_fma_f32 v38, v38, s40, v246\n\t"
    "v_fma_f32 v39, v39, s40, v246\n\t"
    "v_mfma_f32_16x16x32_bf16 v[48:51], v[112:115], a[144:147], v[48:51]\n\t"
    "s_waitcnt lgkmcnt(2)\n\t"
    "v_exp_f32_e32 v36, v36\n\t"
    "v_mfma_f32_16x16x32_bf16 v[48:51], v[116:119], a[148:151], v[48:51]\n\t"
    "s_waitcnt lgkmcnt(1)\n\t"
    "v_exp_f32_e32 v37, v37\n\t"
    "v_mfma_f32_16x16x32_bf16 v[48:51], v[120:123], a[152:155], v[48:51]\n\t"
    "s_waitcnt lgkmcnt(0)\n\t"
    "v_exp_f32_e32 v38, v38\n\t"
    "v_mfma_f32_16x16x32_bf16 v[48:51], v[124:127], a[156:159], v[48:51]\n\t"
    "v_exp_f32_e32 v39, v39\n\t"
    "v_mfma_f32_16x16x32_bf16 v[212:215], v[232:235], v[28:31], v[212:215]\n\t"
    "v_cvt_pk_bf16_f32 v34, v36, v37\n\t"
    "v_cvt_pk_bf16_f32 v35, v38, v39\n\t"
    "v_mfma_f32_16x16x32_bf16 v[52:55], v[128:131], a[128:131], 0\n\t"
    "v_mfma_f32_16x16x32_bf16 v[52:55], v[132:135], a[132:135], v[52:55]\n\t"
    "v_mfma_f32_16x16x32_bf16 v[52:55], v[136:139], a[136:139], v[52:55]\n\t"
    "v_mfma_f32_16x16x32_bf16 v[52:55], v[140:143], a[140:143], v[52:55]\n\t"
    "v_mfma_f32_16x16x32_bf16 v[52:55], v[144:147], a[144:147], v[52:55]\n\t"
    "v_mfma_f32_16x16x32_bf16 v[52:55], v[148:151], a[148:151], v[52:55]\n\t"
    "v_mfma_f32_16x16x32_bf16 v[52:55], v[152:155], a[152:155], v[52:55]\n\t"
    "v_mfma_f32_16x16x32_bf16 v[52:55], v[156:159], a[156:159], v[52:55]\n\t"
    "v_mfma_f32_16x16x32_bf16 v[56:59], v[160:163], a[128:131], 0\n\t"
    "v_mfma_f32_16x16x32_bf16 v[56:59], v[164:167], a[132:135], v[56:59]\n\t"
    "v_mfma_f32_16x16x32_bf16 v[56:59], v[168:171], a[136:139], v[56:59]\n\t"
    "v_max3_f32 v247, v48, v49, v50\n\t"
    "s_nop 3\n\t"
    "v_max3_f32 v247, v247, v51, v52\n\t"
    "v_mfma_f32_16x16x32_bf16 v[56:59], v[172:175], a[140:143], v[56:59]\n\t"
    "v_max3_f32 v247, v247, v53, v54\n\t"
    "v_max_f32_e32 v247, v247, v55\n\t"
    "v_mfma_f32_16x16x32_bf16 v[56:59], v[176:179], a[144:147], v[56:59]\n\t"
    "ds_max_f32 v241, v247 offset:192\n\t"
    "v_mfma_f32_16x16x32_bf16 v[56:59], v[180:183], a[148:151], v[56:59]\n\t"
    "v_mfma_f32_16x16x32_bf16 v[56:59], v[184:187], a[152:155], v[56:59]\n\t"
    "v_mfma_f32_16x16x32_bf16 v[56:59], v[188:191], a[156:159], v[56:59]\n\t"
    "v_mfma_f32_16x16x32_bf16 v[216:219], v[232:235], v[32:35], v[216:219]\n\t"
    "v_mfma_f32_16x16x32_bf16 v[60:63], a[0:3], a[128:131], 0\n\t"
    "v_mfma_f32_16x16x32_bf16 v[60:63], a[4:7], a[132:135], v[60:63]\n\t"
    "v_mfma_f32_16x16x32_bf16 v[60:63], a[8:11], a[136:139], v[60:63]\n\t"
    "v_mfma_f32_16x16x32_bf16 v[60:63], a[12:15], a[140:143], v[60:63]\n\t"
    "v_mfma_f32_16x16x32_bf16 v[60:63], a[16:19], a[144:147], v[60:63]\n\t"
    "v_mfma_f32_16x16x32_bf16 v[60:63], a[20:23], a[148:151], v[60:63]\n\t"
    "v_mfma_f32_16x16x32_bf16 v[60:63], a[24:27], a[152:155], v[60:63]\n\t"
    "v_mfma_f32_16x16x32_bf16 v[60:63], a[28:31], a[156:159], v[60:63]\n\t"
    "s_add_u32 m0, s41, 3328\n\t"
    "s_waitcnt vmcnt(7)\n\t"
    "buffer_load_dwordx4 v240, %2, s58 offen offset:0 nt lds\n\t"
    "s_add_u32 m0, s41, 3360\n\t"
    "s_waitcnt vmcnt(7)\n\t"
    "buffer_load_dwordx4 v240, %2, s58 offen offset:1024 nt lds\n\t"
    "s_add_u32 m0, s41, 3392\n\t"
    "s_waitcnt vmcnt(7)\n\t"
    "buffer_load_dwordx4 v240, %2, s58 offen offset:2048 nt lds\n\t"
    "s_add_u32 m0, s41, 3424\n\t"
    "s_waitcnt vmcnt(7)\n\t"
    "buffer_load_dwordx4 v240, %2, s58 offen offset:3072 nt lds\n\t"
    "s_waitcnt lgkmcnt(0)\n\t"
    "s_barrier\n\t"
    "ds_read_b32 v247, v241 offset:192\n\t"
    "v_add_u32_e32 v238, 0xc600, v238\n\t"
    "ds_read_b128 a[32:35], v238 offset:20224\n\t"
    "ds_read_b128 a[36:39], v238 offset:20288\n\t"
    "ds_read_b128 a[40:43], v238 offset:20352\n\t"
    "ds_read_b128 a[44:47], v238 offset:20416\n\t"
    "ds_read_b128 a[48:51], v238 offset:20480\n\t"
    "ds_read_b128 a[52:55], v238 offset:20544\n\t"
    "ds_read_b128 a[56:59], v238 offset:20608\n\t"
    "ds_read_b128 a[60:63], v238 offset:20672\n\t"
    "s_waitcnt lgkmcnt(8)\n\t"
    "v_mul_f32_e32 v247, 0xbdb8aa3b, v247\n\t"
    "v_fma_f32 v48, v48, s40, v247\n\t"
    "v_fma_f32 v49, v49, s40, v247\n\t"
    "v_fma_f32 v52, v52, s40, v247\n\t"
    "v_fma_f32 v50, v50, s40, v247\n\t"
    "v_fma_f32 v53, v53, s40, v247\n\t"
    "v_fma_f32 v51, v51, s40, v247\n\t"
    "v_fma_f32 v54, v54, s40, v247\n\t"
    "v_fma_f32 v55, v55, s40, v247\n\t"
    "v_exp_f32_e32 v48, v48\n\t"
    "v_exp_f32_e32 v52, v52\n\t"
    "v_exp_f32_e32 v49, v49\n\t"
    "v_exp_f32_e32 v53, v53\n\t"
    "v_exp_f32_e32 v50, v50\n\t"
    "v_exp_f32_e32 v54, v54\n\t"
    "v_exp_f32_e32 v51, v51\n\t"
    "v_exp_f32_e32 v55, v55\n\t"
    "v_cvt_pk_bf16_f32 v14, v52, v53\n\t"
    "v_cvt_pk_bf16_f32 v12, v48, v49\n\t"
    "v_cvt_pk_bf16_f32 v15, v54, v55\n\t"
    "v_fma_f32 v56, v56, s40, v247\n\t"
    "v_cvt_pk_bf16_f32 v13, v50, v51\n\t"
    "v_fma_f32 v57, v57, s40, v247\n\t"
    "s_waitcnt lgkmcnt(7)\n\t"
    "v_mfma_f32_16x16x32_bf16 v[16:19], a[32:35], a[224:227], 0\n\t"
    "s_waitcnt lgkmcnt(6)\n\t"
    "v_fma_f32 v58, v58, s40, v247\n\t"
    "v_fma_f32 v59, v59, s40, v247\n\t"
    "v_mfma_f32_16x16x32_bf16 v[16:19], a[36:39], a[228:231], v[16:19]\n\t"
    "s_waitcnt lgkmcnt(5)\n\t"
    "v_exp_f32_e32 v56, v56\n\t"
    "v_mfma_f32_16x16x32_bf16 v[16:19], a[40:43], a[232:235], v[16:19]\n\t"
    "s_waitcnt lgkmcnt(4)\n\t"
    "v_exp_f32_e32 v57, v57\n\t"
    "v_mfma_f32_16x16x32_bf16 v[16:19], a[44:47], a[236:239], v[16:19]\n\t"
    "s_waitcnt lgkmcnt(0)\n\t"
    "v_exp_f32_e32 v58, v58\n\t"
    "v_mfma_f32_16x16x32_bf16 v[16:19], a[48:51], a[240:243], v[16:19]\n\t"
    "v_exp_f32_e32 v59, v59\n\t"
    "v_mfma_f32_16x16x32_bf16 v[16:19], a[52:55], a[244:247], v[16:19]\n\t"
    "v_cvt_pk_bf16_f32 v36, v56, v57\n\t"
    "v_cvt_pk_bf16_f32 v37, v58, v59\n\t"
    "v_fma_f32 v60, v60, s40, v247\n\t"
    "v_mfma_f32_16x16x32_bf16 v[16:19], a[56:59], a[248:251], v[16:19]\n\t"
    "v_fma_f32 v61, v61, s40, v247\n\t"
    "v_fma_f32 v62, v62, s40, v247\n\t"
    "v_mfma_f32_16x16x32_bf16 v[16:19], a[60:63], a[252:255], v[16:19]\n\t"
    "v_fma_f32 v63, v63, s40, v247\n\t"
    "v_mfma_f32_16x16x32_bf16 v[220:223], v[232:235], v[12:15], 0\n\t"
    "v_exp_f32_e32 v60, v60\n\t"
    "v_mfma_f32_16x16x32_bf16 v[56:59], a[32:35], a[64:67], 0\n\t"
    "v_exp_f32_e32 v61, v61\n\t"
    "v_mfma_f32_16x16x32_bf16 v[56:59], a[36:39], a[68:71], v[56:59]\n\t"
    "v_exp_f32_e32 v62, v62\n\t"
    "v_mfma_f32_16x16x32_bf16 v[56:59], a[40:43], a[72:75], v[56:59]\n\t"
    "v_exp_f32_e32 v63, v63\n\t"
    "v_mfma_f32_16x16x32_bf16 v[56:59], a[44:47], a[76:79], v[56:59]\n\t"
    "v_cvt_pk_bf16_f32 v38, v60, v61\n\t"
    "v_cvt_pk_bf16_f32 v39, v62, v63\n\t"
    "v_mfma_f32_16x16x32_bf16 v[56:59], a[48:51], a[80:83], v[56:59]\n\t"
    "v_fma_f32 v16, v16, s40, v244\n\t"
    "v_fma_f32 v17, v17, s40, v244\n\t"
    "v_mfma_f32_16x16x32_bf16 v[56:59], a[52:55], a[84:87], v[56:59]\n\t"
    "v_fma_f32 v18, v18, s40, v244\n\t"
    "v_fma_f32 v19, v19, s40, v244\n\t"
    "v_mfma_f32_16x16x32_bf16 v[56:59], a[56:59], a[88:91], v[56:59]\n\t"
    "v_exp_f32_e32 v16, v16\n\t"
    "v_mfma_f32_16x16x32_bf16 v[56:59], a[60:63], a[92:95], v[56:59]\n\t"
    "s_add_u32 m0, s41, 54016\n\t"
    "s_waitcnt vmcnt(7)\n\t"
    "buffer_load_dwordx4 v240, %1, s52 offen offset:0 nt lds\n\t"
    "s_add_u32 m0, s41, 54048\n\t"
    "s_waitcnt vmcnt(7)\n\t"
    "buffer_load_dwordx4 v240, %1, s52 offen offset:1024 nt lds\n\t"
    "s_add_u32 m0, s41, 54080\n\t"
    "s_waitcnt vmcnt(7)\n\t"
    "buffer_load_dwordx4 v240, %1, s52 offen offset:2048 nt lds\n\t"
    "s_add_u32 m0, s41, 54112\n\t"
    "s_waitcnt vmcnt(7)\n\t"
    "buffer_load_dwordx4 v240, %1, s52 offen offset:3072 nt lds\n\t"
    "PG1_%=:\n\t"
    "s_cbranch_execz PG5_%=\n\t"
    "s_barrier\n\t"
    "v_exp_f32_e32 v17, v17\n\t"
    "v_exp_f32_e32 v18, v18\n\t"
    "v_exp_f32_e32 v19, v19\n\t"
    "v_cvt_pk_bf16_f32 v48, v16, v17\n\t"
    "v_mfma_f32_16x16x32_bf16 v[220:223], v[232:235], v[36:39], v[220:223]\n\t"
    "v_cvt_pk_bf16_f32 v49, v18, v19\n\t"
    "v_add_u32_e32 v239, 0xfffe7400, v239\n\t"
    "v_fma_f32 v56, v56, s40, v245\n\t"
    "v_mfma_f32_16x16x32_bf16 v[60:63], a[32:35], a[96:99], 0\n\t"
    "ds_read_b128 a[192:195], v239 offset:20224\n\t"
    "v_fma_f32 v57, v57, s40, v245\n\t"
    "v_mfma_f32_16x16x32_bf16 v[60:63], a[36:39], a[100:103], v[60:63]\n\t"
    "ds_read_b128 a[196:199], v239 offset:20288\n\t"
    "v_fma_f32 v58, v58, s40, v245\n\t"
    "v_fma_f32 v59, v59, s40, v245\n\t"
    "v_mfma_f32_16x16x32_bf16 v[60:63], a[40:43], a[104:107], v[60:63]\n\t"
    "ds_read_b128 a[200:203], v239 offset:20352\n\t"
    "v_exp_f32_e32 v56, v56\n\t"
    "v_mfma_f32_16x16x32_bf16 v[60:63], a[44:47], a[108:111], v[60:63]\n\t"
    "ds_read_b128 a[204:207], v239 offset:20416\n\t"
    "v_exp_f32_e32 v57, v57\n\t"
    "v_mfma_f32_16x16x32_bf16 v[60:63], a[48:51], a[112:115], v[60:63]\n\t"
    "ds_read_b128 a[208:211], v239 offset:20480\n\t"
    "v_exp_f32_e32 v58, v58\n\t"
    "v_mfma_f32_16x16x32_bf16 v[60:63], a[52:55], a[116:119], v[60:63]\n\t"
    "ds_read_b128 a[212:215], v239 offset:20544\n\t"
    "v_exp_f32_e32 v59, v59\n\t"
    "v_mfma_f32_16x16x32_bf16 v[60:63], a[56:59], a[120:123], v[60:63]\n\t"
    "ds_read_b128 a[216:219], v239 offset:20608\n\t"
    "v_cvt_pk_bf16_f32 v52, v56, v57\n\t"
    "v_cvt_pk_bf16_f32 v53, v58, v59\n\t"
    "v_mfma_f32_16x16x32_bf16 v[60:63], a[60:63], a[124:127], v[60:63]\n\t"
    "ds_read_b128 a[220:223], v239 offset:20672\n\t"
    "v_mfma_f32_16x16x32_bf16 v[16:19], a[32:35], a[128:131], 0\n\t"
    "v_mfma_f32_16x16x32_bf16 v[16:19], a[36:39], a[132:135], v[16:19]\n\t"
    "v_mfma_f32_16x16x32_bf16 v[16:19], a[40:43], a[136:139], v[16:19]\n\t"
    "v_mfma_f32_16x16x32_bf16 v[16:19], a[44:47], a[140:143], v[16:19]\n\t"
    "s_nop 2\n\t"
    "v_fma_f32 v60, v60, s40, v246\n\t"
    "v_fma_f32 v61, v61, s40, v246\n\t"
    "v_mfma_f32_16x16x32_bf16 v[16:19], a[48:51], a[144:147], v[16:19]\n\t"
    "v_fma_f32 v62, v62, s40, v246\n\t"
    "v_fma_f32 v63, v63, s40, v246\n\t"
    "v_mfma_f32_16x16x32_bf16 v[16:19], a[52:55], a[148:151], v[16:19]\n\t"
    "v_exp_f32_e32 v60, v60\n\t"
    "v_mfma_f32_16x16x32_bf16 v[16:19], a[56:59], a[152:155], v[16:19]\n\t"
    "v_exp_f32_e32 v61, v61\n\t"
    "v_mfma_f32_16x16x32_bf16 v[16:19], a[60:63], a[156:159], v[16:19]\n\t"
    "s_waitcnt lgkmcnt(4)\n\t"
    "v_exp_f32_e32 v62, v62\n\t"
    "v_mfma_f32_16x16x32_bf16 v[40:43], v[96:99], a[192:195], 0\n\t"
    "v_exp_f32_e32 v63, v63\n\t"
    "v_mfma_f32_16x16x32_bf16 v[40:43], v[100:103], a[196:199], v[40:43]\n\t"
    "v_cvt_pk_bf16_f32 v56, v60, v61\n\t"
    "v_cvt_pk_bf16_f32 v57, v62, v63\n\t"
    "v_mfma_f32_16x16x32_bf16 v[40:43], v[104:107], a[200:203], v[40:43]\n\t"
    "v_mfma_f32_16x16x32_bf16 v[40:43], v[108:111], a[204:207], v[40:43]\n\t"
    "s_waitcnt lgkmcnt(1)\n\t"
    "v_fma_f32 v16, v16, s40, v247\n\t"
    "v_fma_f32 v17, v17, s40, v247\n\t"
    "v_mfma_f32_16x16x32_bf16 v[40:43], v[112:115], a[208:211], v[40:43]\n\t"
    "v_fma_f32 v18, v18, s40, v247\n\t"
    "v_fma_f32 v19, v19, s40, v247\n\t"
    "v_mfma_f32_16x16x32_bf16 v[40:43], v[116:119], a[212:215], v[40:43]\n\t"
    "v_exp_f32_e32 v16, v16\n\t"
    "v_mfma_f32_16x16x32_bf16 v[40:43], v[120:123], a[216:219], v[40:43]\n\t"
    "s_waitcnt lgkmcnt(0)\n\t"
    "v_exp_f32_e32 v17, v17\n\t"
    "v_mfma_f32_16x16x32_bf16 v[40:43], v[124:127], a[220:223], v[40:43]\n\t"
    "v_exp_f32_e32 v18, v18\n\t"
    "v_mfma_f32_16x16x32_bf16 v[64:67], v[128:131], a[192:195], 0\n\t"
    "v_exp_f32_e32 v19, v19\n\t"
    "v_mfma_f32_16x16x32_bf16 v[64:67], v[132:135], a[196:199], v[64:67]\n\t"
    "v_cvt_pk_bf16_f32 v60, v16, v17\n\t"
    "v_cvt_pk_bf16_f32 v61, v18, v19\n\t"
    "v_mfma_f32_16x16x32_bf16 v[64:67], v[136:139], a[200:203], v[64:67]\n\t"
    "v_mfma_f32_16x16x32_bf16 v[64:67], v[140:143], a[204:207], v[64:67]\n\t"
    "v_mfma_f32_16x16x32_bf16 v[64:67], v[144:147], a[208:211], v[64:67]\n\t"
    "v_mfma_f32_16x16x32_bf16 v[64:67], v[148:151], a[212:215], v[64:67]\n\t"
    "v_mfma_f32_16x16x32_bf16 v[64:67], v[152:155], a[216:219], v[64:67]\n\t"
    "v_mfma_f32_16x16x32_bf16 v[64:67], v[156:159], a[220:223], v[64:67]\n\t"
    "v_mfma_f32_16x16x32_bf16 v[44:47], v[160:163], a[192:195], 0\n\t"
    "v_mfma_f32_16x16x32_bf16 v[44:47], v[164:167], a[196:199], v[44:47]\n\t"
    "v_mfma_f32_16x16x32_bf16 v[44:47], v[168:171], a[200:203], v[44:47]\n\t"
    "v_max3_f32 v248, v40, v41, v42\n\t"
    "s_nop 3\n\t"
    "v_max3_f32 v248, v248, v43, v64\n\t"
    "v_mfma_f32_16x16x32_bf16 v[44:47], v[172:175], a[204:207], v[44:47]\n\t"
    "v_max3_f32 v248, v248, v65, v66\n\t"
    "v_max_f32_e32 v248, v248, v67\n\t"
    "v_mfma_f32_16x16x32_bf16 v[44:47], v[176:179], a[208:211], v[44:47]\n\t"
    "ds_max_f32 v241, v248 offset:256\n\t"
    "v_mfma_f32_16x16x32_bf16 v[44:47], v[180:183], a[212:215], v[44:47]\n\t"
    "v_mfma_f32_16x16x32_bf16 v[44:47], v[184:187], a[216:219], v[44:47]\n\t"
    "v_mfma_f32_16x16x32_bf16 v[44:47], v[188:191], a[220:223], v[44:47]\n\t"
    "v_mfma_f32_16x16x32_bf16 v[68:71], a[0:3], a[192:195], 0\n\t"
    "v_mfma_f32_16x16x32_bf16 v[68:71], a[4:7], a[196:199], v[68:71]\n\t"
    "v_mfma_f32_16x16x32_bf16 v[68:71], a[8:11], a[200:203], v[68:71]\n\t"
    "v_mfma_f32_16x16x32_bf16 v[68:71], a[12:15], a[204:207], v[68:71]\n\t"
    "v_mfma_f32_16x16x32_bf16 v[68:71], a[16:19], a[208:211], v[68:71]\n\t"
    "v_mfma_f32_16x16x32_bf16 v[68:71], a[20:23], a[212:215], v[68:71]\n\t"
    "v_mfma_f32_16x16x32_bf16 v[68:71], a[24:27], a[216:219], v[68:71]\n\t"
    "v_mfma_f32_16x16x32_bf16 v[68:71], a[28:31], a[220:223], v[68:71]\n\t"
    "s_add_u32 m0, s41, 104704\n\t"
    "s_waitcnt vmcnt(7)\n\t"
    "buffer_load_dwordx4 v240, %2, s59 offen offset:0 nt lds\n\t"
    "s_add_u32 m0, s41, 104736\n\t"
    "s_waitcnt vmcnt(7)\n\t"
    "buffer_load_dwordx4 v240, %2, s59 offen offset:1024 nt lds\n\t"
    "s_add_u32 m0, s41, 104768\n\t"
    "s_waitcnt vmcnt(7)\n\t"
    "buffer_load_dwordx4 v240, %2, s59 offen offset:2048 nt lds\n\t"
    "s_add_u32 m0, s41, 104800\n\t"
    "s_waitcnt vmcnt(7)\n\t"
    "buffer_load_dwordx4 v240, %2, s59 offen offset:3072 nt lds\n\t"
    "s_waitcnt lgkmcnt(0)\n\t"
    "s_barrier\n\t"
    "ds_read_b32 v248, v241 offset:256\n\t"
    "v_add_u32_e32 v238, 0xfffe7400, v238\n\t"
    "v_mfma_f32_16x16x32_bf16 v[20:23], a[32:35], a[192:195], 0\n\t"
    "ds_read_b128 a[160:163], v238 offset:3328\n\t"
    "v_mfma_f32_16x16x32_bf16 v[20:23], a[36:39], a[196:199], v[20:23]\n\t"
    "ds_read_b128 a[164:167], v238 offset:3392\n\t"
    "v_mfma_f32_16x16x32_bf16 v[20:23], a[40:43], a[200:203], v[20:23]\n\t"
    "ds_read_b128 a[168:171], v238 offset:3456\n\t"
    "v_mfma_f32_16x16x32_bf16 v[20:23], a[44:47], a[204:207], v[20:23]\n\t"
    "ds_read_b128 a[172:175], v238 offset:3520\n\t"
    "v_mfma_f32_16x16x32_bf16 v[20:23], a[48:51], a[208:211], v[20:23]\n\t"
    "ds_read_b128 a[176:179], v238 offset:3584\n\t"
    "v_mfma_f32_16x16x32_bf16 v[20:23], a[52:55], a[212:215], v[20:23]\n\t"
    "ds_read_b128 a[180:183], v238 offset:3648\n\t"
    "ds_read_b128 a[184:187], v238 offset:3712\n\t"
    "ds_read_b128 a[188:191], v238 offset:3776\n\t"
    "v_mfma_f32_16x16x32_bf16 v[20:23], a[56:59], a[216:219], v[20:23]\n\t"
    "v_mfma_f32_16x16x32_bf16 v[20:23], a[60:63], a[220:223], v[20:23]\n\t"
    "s_waitcnt lgkmcnt(8)\n\t"
    "v_mul_f32_e32 v248, 0xbdb8aa3b, v248\n\t"
    "v_fma_f32 v40, v40, s40, v248\n\t"
    "s_waitcnt lgkmcnt(7)\n\t"
    "v_mfma_f32_16x16x32_bf16 v[76:79], a[160:163], a[224:227], 0\n\t"
    "s_waitcnt lgkmcnt(6)\n\t"
    "v_fma_f32 v41, v41, s40, v248\n\t"
    "v_fma_f32 v42, v42, s40, v248\n\t"
    "v_mfma_f32_16x16x32_bf16 v[76:79], a[164:167], a[228:231], v[76:79]\n\t"
    "s_waitcnt lgkmcnt(5)\n\t"
    "v_fma_f32 v43, v43, s40, v248\n\t"
    "v_mfma_f32_16x16x32_bf16 v[76:79], a[168:171], a[232:235], v[76:79]\n\t"
    "s_waitcnt lgkmcnt(4)\n\t"
    "v_exp_f32_e32 v40, v40\n\t"
    "v_mfma_f32_16x16x32_bf16 v[76:79], a[172:175], a[236:239], v[76:79]\n\t"
    "s_waitcnt lgkmcnt(3)\n\t"
    "v_exp_f32_e32 v41, v41\n\t"
    "v_mfma_f32_16x16x32_bf16 v[76:79], a[176:179], a[240:243], v[76:79]\n\t"
    "s_waitcnt lgkmcnt(2)\n\t"
    "v_exp_f32_e32 v42, v42\n\t"
    "v_mfma_f32_16x16x32_bf16 v[76:79], a[180:183], a[244:247], v[76:79]\n\t"
    "s_waitcnt lgkmcnt(1)\n\t"
    "v_exp_f32_e32 v43, v43\n\t"
    "v_mfma_f32_16x16x32_bf16 v[76:79], a[184:187], a[248:251], v[76:79]\n\t"
    "s_waitcnt lgkmcnt(0)\n\t"
    "v_cvt_pk_bf16_f32 v16, v40, v41\n\t"
    "v_cvt_pk_bf16_f32 v17, v42, v43\n\t"
    "v_mfma_f32_16x16x32_bf16 v[76:79], a[188:191], a[252:255], v[76:79]\n\t"
    "v_fma_f32 v64, v64, s40, v248\n\t"
    "v_fma_f32 v65, v65, s40, v248\n\t"
    "v_mfma_f32_16x16x32_bf16 v[80:83], a[160:163], a[64:67], 0\n\t"
    "v_fma_f32 v66, v66, s40, v248\n\t"
    "v_fma_f32 v67, v67, s40, v248\n\t"
    "v_mfma_f32_16x16x32_bf16 v[80:83], a[164:167], a[68:71], v[80:83]\n\t"
    "v_exp_f32_e32 v64, v64\n\t"
    "v_mfma_f32_16x16x32_bf16 v[80:83], a[168:171], a[72:75], v[80:83]\n\t"
    "v_exp_f32_e32 v65, v65\n\t"
    "v_mfma_f32_16x16x32_bf16 v[80:83], a[172:175], a[76:79], v[80:83]\n\t"
    "v_exp_f32_e32 v66, v66\n\t"
    "v_mfma_f32_16x16x32_bf16 v[80:83], a[176:179], a[80:83], v[80:83]\n\t"
    "v_exp_f32_e32 v67, v67\n\t"
    "v_mfma_f32_16x16x32_bf16 v[80:83], a[180:183], a[84:87], v[80:83]\n\t"
    "v_cvt_pk_bf16_f32 v18, v64, v65\n\t"
    "v_cvt_pk_bf16_f32 v19, v66, v67\n\t"
    "v_mfma_f32_16x16x32_bf16 v[80:83], a[184:187], a[88:91], v[80:83]\n\t"
    "v_fma_f32 v44, v44, s40, v248\n\t"
    "v_fma_f32 v45, v45, s40, v248\n\t"
    "v_mfma_f32_16x16x32_bf16 v[80:83], a[188:191], a[92:95], v[80:83]\n\t"
    "v_fma_f32 v46, v46, s40, v248\n\t"
    "v_fma_f32 v47, v47, s40, v248\n\t"
    "v_mfma_f32_16x16x32_bf16 v[84:87], a[160:163], a[96:99], 0\n\t"
    "v_exp_f32_e32 v44, v44\n\t"
    "v_mfma_f32_16x16x32_bf16 v[84:87], a[164:167], a[100:103], v[84:87]\n\t"
    "v_exp_f32_e32 v45, v45\n\t"
    "v_mfma_f32_16x16x32_bf16 v[84:87], a[168:171], a[104:107], v[84:87]\n\t"
    "v_exp_f32_e32 v46, v46\n\t"
    "v_mfma_f32_16x16x32_bf16 v[84:87], a[172:175], a[108:111], v[84:87]\n\t"
    "v_exp_f32_e32 v47, v47\n\t"
    "v_mfma_f32_16x16x32_bf16 v[84:87], a[176:179], a[112:115], v[84:87]\n\t"
    "v_cvt_pk_bf16_f32 v40, v44, v45\n\t"
    "v_cvt_pk_bf16_f32 v41, v46, v47\n\t"
    "v_mfma_f32_16x16x32_bf16 v[84:87], a[180:183], a[116:119], v[84:87]\n\t"
    "v_fma_f32 v68, v68, s40, v248\n\t"
    "v_fma_f32 v69, v69, s40, v248\n\t"
    "v_mfma_f32_16x16x32_bf16 v[84:87], a[184:187], a[120:123], v[84:87]\n\t"
    "v_fma_f32 v70, v70, s40, v248\n\t"
    "v_fma_f32 v71, v71, s40, v248\n\t"
    "v_mfma_f32_16x16x32_bf16 v[84:87], a[188:191], a[124:127], v[84:87]\n\t"
    "s_add_u32 m0, s41, 121600\n\t"
    "s_waitcnt vmcnt(7)\n\t"
    "buffer_load_dwordx4 v240, %2, s60 offen offset:0 nt lds\n\t"
    "s_add_u32 m0, s41, 121632\n\t"
    "s_waitcnt vmcnt(7)\n\t"
    "buffer_load_dwordx4 v240, %2, s60 offen offset:1024 nt lds\n\t"
    "s_add_u32 m0, s41, 121664\n\t"
    "s_waitcnt vmcnt(7)\n\t"
    "buffer_load_dwordx4 v240, %2, s60 offen offset:2048 nt lds\n\t"
    "s_add_u32 m0, s41, 121696\n\t"
    "s_waitcnt vmcnt(7)\n\t"
    "buffer_load_dwordx4 v240, %2, s60 offen offset:3072 nt lds\n\t"
    "s_barrier\n\t"
    "v_exp_f32_e32 v68, v68\n\t"
    "v_exp_f32_e32 v69, v69\n\t"
    "v_exp_f32_e32 v70, v70\n\t"
    "v_exp_f32_e32 v71, v71\n\t"
    "v_cvt_pk_bf16_f32 v42, v68, v69\n\t"
    "v_cvt_pk_bf16_f32 v43, v70, v71\n\t"
    "v_fma_f32 v20, v20, s40, v248\n\t"
    "v_fma_f32 v21, v21, s40, v248\n\t"
    "v_fma_f32 v22, v22, s40, v248\n\t"
    "v_fma_f32 v23, v23, s40, v248\n\t"
    "v_exp_f32_e32 v20, v20\n\t"
    "v_exp_f32_e32 v21, v21\n\t"
    "v_mfma_f32_16x16x32_bf16 v[224:227], v[232:235], v[16:19], 0\n\t"
    "v_exp_f32_e32 v22, v22\n\t"
    "v_exp_f32_e32 v23, v23\n\t"
    "v_mfma_f32_16x16x32_bf16 v[224:227], v[232:235], v[40:43], v[224:227]\n\t"
    "v_cvt_pk_bf16_f32 v64, v20, v21\n\t"
    "v_add_u32_e32 v239, 0xc600, v239\n\t"
    "v_cvt_pk_bf16_f32 v65, v22, v23\n\t"
    "v_mfma_f32_16x16x32_bf16 v[44:47], a[160:163], a[128:131], 0\n\t"
    "ds_read_b128 a[224:227], v239 offset:3328\n\t"
    "v_fma_f32 v76, v76, s40, v244\n\t"
    "v_fma_f32 v77, v77, s40, v244\n\t"
    "v_mfma_f32_16x16x32_bf16 v[44:47], a[164:167], a[132:135], v[44:47]\n\t"
    "ds_read_b128 a[228:231], v239 offset:3392\n\t"
    "v_fma_f32 v78, v78, s40, v244\n\t"
    "v_fma_f32 v79, v79, s40, v244\n\t"
    "v_mfma_f32_16x16x32_bf16 v[44:47], a[168:171], a[136:139], v[44:47]\n\t"
    "ds_read_b128 a[232:235], v239 offset:3456\n\t"
    "v_exp_f32_e32 v76, v76\n\t"
    "v_mfma_f32_16x16x32_bf16 v[44:47], a[172:175], a[140:143], v[44:47]\n\t"
    "ds_read_b128 a[236:239], v239 offset:3520\n\t"
    "v_exp_f32_e32 v77, v77\n\t"
    "v_mfma_f32_16x16x32_bf16 v[44:47], a[176:179], a[144:147], v[44:47]\n\t"
    "ds_read_b128 a[240:243], v239 offset:3584\n\t"
    "v_exp_f32_e32 v78, v78\n\t"
    "v_mfma_f32_16x16x32_bf16 v[44:47], a[180:183], a[148:151], v[44:47]\n\t"
    "s_add_u32 m0, s41, 87808\n\t"
    "ds_read_b128 a[244:247], v239 offset:3648\n\t"
    "v_exp_f32_e32 v79, v79\n\t"
    "v_mfma_f32_16x16x32_bf16 v[44:47], a[184:187], a[152:155], v[44:47]\n\t"
    "ds_read_b128 a[248:251], v239 offset:3712\n\t"
    "v_cvt_pk_bf16_f32 v50, v76, v77\n\t"
    "v_cvt_pk_bf16_f32 v51, v78, v79\n\t"
    "v_mfma_f32_16x16x32_bf16 v[44:47], a[188:191], a[156:159], v[44:47]\n\t"
    "ds_read_b128 a[252:255], v239 offset:3776\n\t"
    "v_fma_f32 v80, v80, s40, v245\n\t"
    "v_fma_f32 v81, v81, s40, v245\n\t"
    "v_mfma_f32_16x16x32_bf16 v[68:71], a[160:163], a[192:195], 0\n\t"
    "v_fma_f32 v82, v82, s40, v245\n\t"
    "v_fma_f32 v83, v83, s40, v245\n\t"
    "v_mfma_f32_16x16x32_bf16 v[68:71], a[164:167], a[196:199], v[68:71]\n\t"
    "v_exp_f32_e32 v80, v80\n\t"
    "v_mfma_f32_16x16x32_bf16 v[68:71], a[168:171], a[200:203], v[68:71]\n\t"
    "v_exp_f32_e32 v81, v81\n\t"
    "v_mfma_f32_16x16x32_bf16 v[68:71], a[172:175], a[204:207], v[68:71]\n\t"
    "v_exp_f32_e32 v82, v82\n\t"
    "v_mfma_f32_16x16x32_bf16 v[68:71], a[176:179], a[208:211], v[68:71]\n\t"
    "v_exp_f32_e32 v83, v83\n\t"
    "v_cvt_pk_bf16_f32 v54, v80, v81\n\t"
    "v_mfma_f32_16x16x32_bf16 v[68:71], a[180:183], a[212:215], v[68:71]\n\t"
    "v_cvt_pk_bf16_f32 v55, v82, v83\n\t"
    "v_fma_f32 v84, v84, s40, v246\n\t"
    "v_fma_f32 v85, v85, s40, v246\n\t"
    "v_mfma_f32_16x16x32_bf16 v[68:71], a[184:187], a[216:219], v[68:71]\n\t"
    "v_fma_f32 v86, v86, s40, v246\n\t"
    "v_fma_f32 v87, v87, s40, v246\n\t"
    "v_mfma_f32_16x16x32_bf16 v[68:71], a[188:191], a[220:223], v[68:71]\n\t"
    "v_exp_f32_e32 v84, v84\n\t"
    "v_mfma_f32_16x16x32_bf16 v[208:211], v[232:235], v[48:51], v[208:211]\n\t"
    "s_waitcnt lgkmcnt(1)\n\t"
    "v_exp_f32_e32 v85, v85\n\t"
    "v_mfma_f32_16x16x32_bf16 v[20:23], a[0:3], a[224:227], 0\n\t"
    "v_exp_f32_e32 v86, v86\n\t"
    "v_mfma_f32_16x16x32_bf16 v[20:23], a[4:7], a[228:231], v[20:23]\n\t"
    "buffer_load_dwordx4 v240, %2, s61 offen offset:0 nt lds\n\t"
    "v_exp_f32_e32 v87, v87\n\t"
    "v_mfma_f32_16x16x32_bf16 v[20:23], a[8:11], a[232:235], v[20:23]\n\t"
    "v_cvt_pk_bf16_f32 v58, v84, v85\n\t"
    "v_cvt_pk_bf16_f32 v59, v86, v87\n\t"
    "v_mfma_f32_16x16x32_bf16 v[20:23], a[12:15], a[236:239], v[20:23]\n\t"
    "v_fma_f32 v44, v44, s40, v247\n\t"
    "v_fma_f32 v45, v45, s40, v247\n\t"
    "v_mfma_f32_16x16x32_bf16 v[20:23], a[16:19], a[240:243], v[20:23]\n\t"
    "v_fma_f32 v46, v46, s40, v247\n\t"
    "v_fma_f32 v47, v47, s40, v247\n\t"
    "v_mfma_f32_16x16x32_bf16 v[20:23], a[20:23], a[244:247], v[20:23]\n\t"
    "v_exp_f32_e32 v44, v44\n\t"
    "v_mfma_f32_16x16x32_bf16 v[20:23], a[24:27], a[248:251], v[20:23]\n\t"
    "s_waitcnt lgkmcnt(0)\n\t"
    "v_exp_f32_e32 v45, v45\n\t"
    "v_mfma_f32_16x16x32_bf16 v[20:23], a[28:31], a[252:255], v[20:23]\n\t"
    "v_exp_f32_e32 v46, v46\n\t"
    "v_mfma_f32_16x16x32_bf16 v[212:215], v[232:235], v[52:55], v[212:215]\n\t"
    "v_exp_f32_e32 v47, v47\n\t"
    "v_mfma_f32_16x16x32_bf16 v[216:219], v[232:235], v[56:59], v[216:219]\n\t"
    "v_cvt_pk_bf16_f32 v62, v44, v45\n\t"
    "v_cvt_pk_bf16_f32 v63, v46, v47\n\t"
    "v_mfma_f32_16x16x32_bf16 v[76:79], a[32:35], a[224:227], 0\n\t"
    "v_fma_f32 v68, v68, s40, v248\n\t"
    "v_fma_f32 v69, v69, s40, v248\n\t"
    "v_mfma_f32_16x16x32_bf16 v[76:79], a[36:39], a[228:231], v[76:79]\n\t"
    "v_fma_f32 v70, v70, s40, v248\n\t"
    "v_fma_f32 v71, v71, s40, v248\n\t"
    "v_mfma_f32_16x16x32_bf16 v[76:79], a[40:43], a[232:235], v[76:79]\n\t"
    "v_exp_f32_e32 v68, v68\n\t"
    "v_mfma_f32_16x16x32_bf16 v[76:79], a[44:47], a[236:239], v[76:79]\n\t"
    "s_add_u32 m0, s41, 87840\n\t"
    "v_exp_f32_e32 v69, v69\n\t"
    "v_mfma_f32_16x16x32_bf16 v[76:79], a[48:51], a[240:243], v[76:79]\n\t"
    "v_exp_f32_e32 v70, v70\n\t"
    "v_mfma_f32_16x16x32_bf16 v[76:79], a[52:55], a[244:247], v[76:79]\n\t"
    "v_exp_f32_e32 v71, v71\n\t"
    "v_mfma_f32_16x16x32_bf16 v[76:79], a[56:59], a[248:251], v[76:79]\n\t"
    "v_cvt_pk_bf16_f32 v66, v68, v69\n\t"
    "v_cvt_pk_bf16_f32 v67, v70, v71\n\t"
    "v_mfma_f32_16x16x32_bf16 v[76:79], a[60:63], a[252:255], v[76:79]\n\t"
    "v_mfma_f32_16x16x32_bf16 v[220:223], v[232:235], v[60:63], v[220:223]\n\t"
    "v_mfma_f32_16x16x32_bf16 v[80:83], a[160:163], a[224:227], 0\n\t"
    "v_mfma_f32_16x16x32_bf16 v[80:83], a[164:167], a[228:231], v[80:83]\n\t"
    "v_max3_f32 v249, v20, v21, v22\n\t"
    "s_nop 3\n\t"
    "v_max3_f32 v249, v249, v23, v76\n\t"
    "v_mfma_f32_16x16x32_bf16 v[80:83], a[168:171], a[232:235], v[80:83]\n\t"
    "v_max3_f32 v249, v249, v77, v78\n\t"
    "v_max_f32_e32 v249, v249, v79\n\t"
    "v_mfma_f32_16x16x32_bf16 v[80:83], a[172:175], a[236:239], v[80:83]\n\t"
    "ds_max_f32 v241, v249 offset:320\n\t"
    "v_mfma_f32_16x16x32_bf16 v[80:83], a[176:179], a[240:243], v[80:83]\n\t"
    "v_mfma_f32_16x16x32_bf16 v[80:83], a[180:183], a[244:247], v[80:83]\n\t"
    "v_mfma_f32_16x16x32_bf16 v[80:83], a[184:187], a[248:251], v[80:83]\n\t"
    "v_mfma_f32_16x16x32_bf16 v[80:83], a[188:191], a[252:255], v[80:83]\n\t"
    "buffer_load_dwordx4 v240, %2, s61 offen offset:1024 nt lds\n\t"
    "v_mfma_f32_16x16x32_bf16 v[224:227], v[232:235], v[64:67], v[224:227]\n\t"
    "v_mfma_f32_16x16x32_bf16 v[84:87], v[96:99], a[224:227], 0\n\t"
    "v_mfma_f32_16x16x32_bf16 v[84:87], v[100:103], a[228:231], v[84:87]\n\t"
    "v_mfma_f32_16x16x32_bf16 v[84:87], v[104:107], a[232:235], v[84:87]\n\t"
    "v_mfma_f32_16x16x32_bf16 v[84:87], v[108:111], a[236:239], v[84:87]\n\t"
    "v_mfma_f32_16x16x32_bf16 v[84:87], v[112:115], a[240:243], v[84:87]\n\t"
    "v_mfma_f32_16x16x32_bf16 v[84:87], v[116:119], a[244:247], v[84:87]\n\t"
    "v_mfma_f32_16x16x32_bf16 v[84:87], v[120:123], a[248:251], v[84:87]\n\t"
    "v_mfma_f32_16x16x32_bf16 v[84:87], v[124:127], a[252:255], v[84:87]\n\t"
    "s_waitcnt vmcnt(6) lgkmcnt(0)\n\t"
    "s_barrier\n\t"
    "ds_read_b32 v249, v241 offset:320\n\t"
    "v_add_u32_e32 v238, 0x18c00, v238\n\t"
    "v_mfma_f32_16x16x32_bf16 v[88:91], v[128:131], a[224:227], 0\n\t"
    "s_add_u32 m0, s41, 87872\n\t"
    "ds_read_b128 a[0:3], v238 offset:3328\n\t"
    "v_mfma_f32_16x16x32_bf16 v[88:91], v[132:135], a[228:231], v[88:91]\n\t"
    "ds_read_b128 a[4:7], v238 offset:3392\n\t"
    "v_mfma_f32_16x16x32_bf16 v[88:91], v[136:139], a[232:235], v[88:91]\n\t"
    "ds_read_b128 a[8:11], v238 offset:3456\n\t"
    "v_mfma_f32_16x16x32_bf16 v[88:91], v[140:143], a[236:239], v[88:91]\n\t"
    "ds_read_b128 a[12:15], v238 offset:3520\n\t"
    "v_mfma_f32_16x16x32_bf16 v[88:91], v[144:147], a[240:243], v[88:91]\n\t"
    "ds_read_b128 a[16:19], v238 offset:3584\n\t"
    "v_mfma_f32_16x16x32_bf16 v[88:91], v[148:151], a[244:247], v[88:91]\n\t"
    "buffer_load_dwordx4 v240, %2, s61 offen offset:2048 nt lds\n\t"
    "v_mfma_f32_16x16x32_bf16 v[88:91], v[152:155], a[248:251], v[88:91]\n\t"
    "ds_read_b128 a[20:23], v238 offset:3648\n\t"
    "v_mfma_f32_16x16x32_bf16 v[88:91], v[156:159], a[252:255], v[88:91]\n\t"
    "s_add_u32 m0, s41, 87904\n\t"
    "ds_read_b128 a[24:27], v238 offset:3712\n\t"
    "s_waitcnt lgkmcnt(7)\n\t"
    "v_mul_f32_e32 v249, 0xbdb8aa3b, v249\n\t"
    "v_mfma_f32_16x16x32_bf16 v[92:95], v[160:163], a[224:227], 0\n\t"
    "ds_read_b128 a[28:31], v238 offset:3776\n\t"
    "v_add_u32_e32 v239, 0xffff3a00, v239\n\t"
    "v_mfma_f32_16x16x32_bf16 v[92:95], v[164:167], a[228:231], v[92:95]\n\t"
    "v_fma_f32 v20, v20, s40, v249\n\t"
    "v_fma_f32 v21, v21, s40, v249\n\t"
    "v_mfma_f32_16x16x32_bf16 v[92:95], v[168:171], a[232:235], v[92:95]\n\t"
    "ds_read_b128 a[32:35], v239 offset:37120\n\t"
    "v_fma_f32 v22, v22, s40, v249\n\t"
    "v_mfma_f32_16x16x32_bf16 v[92:95], v[172:175], a[236:239], v[92:95]\n\t"
    "ds_read_b128 a[36:39], v239 offset:37184\n\t"
    "v_fma_f32 v23, v23, s40, v249\n\t"
    "v_mfma_f32_16x16x32_bf16 v[92:95], v[176:179], a[240:243], v[92:95]\n\t"
    "buffer_load_dwordx4 v240, %2, s61 offen offset:3072 nt lds\n\t"
    "v_exp_f32_e32 v20, v20\n\t"
    "v_mfma_f32_16x16x32_bf16 v[92:95], v[180:183], a[244:247], v[92:95]\n\t"
    "ds_read_b128 a[40:43], v239 offset:37248\n\t"
    "v_exp_f32_e32 v21, v21\n\t"
    "v_mfma_f32_16x16x32_bf16 v[92:95], v[184:187], a[248:251], v[92:95]\n\t"
    "ds_read_b128 a[44:47], v239 offset:37312\n\t"
    "v_exp_f32_e32 v22, v22\n\t"
    "v_mfma_f32_16x16x32_bf16 v[92:95], v[188:191], a[252:255], v[92:95]\n\t"
    "s_waitcnt vmcnt(4)\n\t"
    "s_barrier\n\t"
    "ds_read_b128 a[48:51], v239 offset:37376\n\t"
    "s_waitcnt lgkmcnt(8)\n\t"
    "v_mfma_f32_16x16x32_bf16 v[72:75], a[0:3], a[64:67], 0\n\t"
    "ds_read_b128 a[52:55], v239 offset:37440\n\t"
    "v_exp_f32_e32 v23, v23\n\t"
    "v_cvt_pk_bf16_f32 v46, v20, v21\n\t"
    "v_mfma_f32_16x16x32_bf16 v[72:75], a[4:7], a[68:71], v[72:75]\n\t"
    "s_add_u32 m0, s41, 138496\n\t"
    "v_cvt_pk_bf16_f32 v47, v22, v23\n\t"
    "v_fma_f32 v76, v76, s40, v249\n\t"
    "v_fma_f32 v77, v77, s40, v249\n\t"
    "v_mfma_f32_16x16x32_bf16 v[72:75], a[8:11], a[72:75], v[72:75]\n\t"
    "ds_read_b128 a[56:59], v239 offset:37504\n\t"
    "v_fma_f32 v78, v78, s40, v249\n\t"
    "v_fma_f32 v79, v79, s40, v249\n\t"
    "v_mfma_f32_16x16x32_bf16 v[72:75], a[12:15], a[76:79], v[72:75]\n\t"
    "buffer_load_dwordx4 v240, %2, s62 offen offset:0 nt lds\n\t"
    "v_exp_f32_e32 v76, v76\n\t"
    "v_mfma_f32_16x16x32_bf16 v[72:75], a[16:19], a[80:83], v[72:75]\n\t"
    "s_waitcnt lgkmcnt(5)\n\t"
    "ds_read_b128 a[60:63], v239 offset:37568\n\t"
    "v_exp_f32_e32 v77, v77\n\t"
    "v_mfma_f32_16x16x32_bf16 v[72:75], a[20:23], a[84:87], v[72:75]\n\t"
    "s_add_u32 m0, s41, 138528\n\t"
    "v_exp_f32_e32 v78, v78\n\t"
    "v_mfma_f32_16x16x32_bf16 v[72:75], a[24:27], a[88:91], v[72:75]\n\t"
    "ds_read_b128 a[160:163], v238 offset:20224\n\t"
    "v_exp_f32_e32 v79, v79\n\t"
    "v_mfma_f32_16x16x32_bf16 v[72:75], a[28:31], a[92:95], v[72:75]\n\t"
    "buffer_load_dwordx4 v240, %2, s62 offen offset:1024 nt lds\n\t"
    "v_cvt_pk_bf16_f32 v68, v76, v77\n\t"
    "v_cvt_pk_bf16_f32 v69, v78, v79\n\t"
    "v_mfma_f32_16x16x32_bf16 v[100:103], a[0:3], a[96:99], 0\n\t"
    "v_fma_f32 v80, v80, s40, v249\n\t"
    "v_fma_f32 v81, v81, s40, v249\n\t"
    "v_fma_f32 v82, v82, s40, v249\n\t"
    "v_mfma_f32_16x16x32_bf16 v[100:103], a[4:7], a[100:103], v[100:103]\n\t"
    "s_add_u32 m0, s41, 138560\n\t"
    "ds_read_b128 a[164:167], v238 offset:20288\n\t"
    "v_fma_f32 v83, v83, s40, v249\n\t"
    "v_mfma_f32_16x16x32_bf16 v[100:103], a[8:11], a[104:107], v[100:103]\n\t"
    "v_exp_f32_e32 v80, v80\n\t"
    "v_mfma_f32_16x16x32_bf16 v[100:103], a[12:15], a[108:111], v[100:103]\n\t"
    "buffer_load_dwordx4 v240, %2, s62 offen offset:2048 nt lds\n\t"
    "v_exp_f32_e32 v81, v81\n\t"
    "v_mfma_f32_16x16x32_bf16 v[100:103], a[16:19], a[112:115], v[100:103]\n\t"
    "s_add_u32 m0, s41, 138592\n\t"
    "v_exp_f32_e32 v82, v82\n\t"
    "v_mfma_f32_16x16x32_bf16 v[100:103], a[20:23], a[116:119], v[100:103]\n\t"
    "v_exp_f32_e32 v83, v83\n\t"
    "v_cvt_pk_bf16_f32 v70, v80, v81\n\t"
    "v_mfma_f32_16x16x32_bf16 v[100:103], a[24:27], a[120:123], v[100:103]\n\t"
    "v_cvt_pk_bf16_f32 v71, v82, v83\n\t"
    "v_fma_f32 v84, v84, s40, v249\n\t"
    "v_fma_f32 v85, v85, s40, v249\n\t"
    "v_mfma_f32_16x16x32_bf16 v[100:103], a[28:31], a[124:127], v[100:103]\n\t"
    "ds_read_b128 a[168:171], v238 offset:20352\n\t"
    "v_fma_f32 v86, v86, s40, v249\n\t"
    "v_mfma_f32_16x16x32_bf16 v[104:107], a[0:3], a[128:131], 0\n\t"
    "buffer_load_dwordx4 v240, %2, s62 offen offset:3072 nt lds\n\t"
    "v_fma_f32 v87, v87, s40, v249\n\t"
    "v_mfma_f32_16x16x32_bf16 v[104:107], a[4:7], a[132:135], v[104:107]\n\t"
    "s_add_u32 m0, s41, 3328\n\t"
    "v_exp_f32_e32 v84, v84\n\t"
    "v_mfma_f32_16x16x32_bf16 v[104:107], a[8:11], a[136:139], v[104:107]\n\t"
    "v_exp_f32_e32 v85, v85\n\t"
    "v_mfma_f32_16x16x32_bf16 v[104:107], a[12:15], a[140:143], v[104:107]\n\t"
    "ds_read_b128 a[172:175], v238 offset:20416\n\t"
    "v_exp_f32_e32 v86, v86\n\t"
    "v_mfma_f32_16x16x32_bf16 v[104:107], a[16:19], a[144:147], v[104:107]\n\t"
    "buffer_load_dwordx4 v240, %2, s63 offen offset:0 nt lds\n\t"
    "v_exp_f32_e32 v87, v87\n\t"
    "v_mfma_f32_16x16x32_bf16 v[104:107], a[20:23], a[148:151], v[104:107]\n\t"
    "v_cvt_pk_bf16_f32 v20, v84, v85\n\t"
    "v_cvt_pk_bf16_f32 v21, v86, v87\n\t"
    "v_fma_f32 v88, v88, s40, v249\n\t"
    "v_mfma_f32_16x16x32_bf16 v[104:107], a[24:27], a[152:155], v[104:107]\n\t"
    "s_add_u32 m0, s41, 3360\n\t"
    "v_fma_f32 v89, v89, s40, v249\n\t"
    "v_fma_f32 v90, v90, s40, v249\n\t"
    "v_mfma_f32_16x16x32_bf16 v[104:107], a[28:31], a[156:159], v[104:107]\n\t"
    "ds_read_b128 a[176:179], v238 offset:20480\n\t"
    "v_fma_f32 v91, v91, s40, v249\n\t"
    "v_mfma_f32_16x16x32_bf16 v[108:111], a[0:3], a[192:195], 0\n\t"
    "buffer_load_dwordx4 v240, %2, s63 offen offset:1024 nt lds\n\t"
    "v_exp_f32_e32 v88, v88\n\t"
    "v_mfma_f32_16x16x32_bf16 v[108:111], a[4:7], a[196:199], v[108:111]\n\t"
    "s_add_u32 m0, s41, 3392\n\t"
    "v_exp_f32_e32 v89, v89\n\t"
    "v_mfma_f32_16x16x32_bf16 v[108:111], a[8:11], a[200:203], v[108:111]\n\t"
    "v_exp_f32_e32 v90, v90\n\t"
    "v_mfma_f32_16x16x32_bf16 v[108:111], a[12:15], a[204:207], v[108:111]\n\t"
    "ds_read_b128 a[180:183], v238 offset:20544\n\t"
    "v_exp_f32_e32 v91, v91\n\t"
    "v_mfma_f32_16x16x32_bf16 v[108:111], a[16:19], a[208:211], v[108:111]\n\t"
    "buffer_load_dwordx4 v240, %2, s63 offen offset:2048 nt lds\n\t"
    "v_cvt_pk_bf16_f32 v22, v88, v89\n\t"
    "v_cvt_pk_bf16_f32 v23, v90, v91\n\t"
    "v_mfma_f32_16x16x32_bf16 v[108:111], a[20:23], a[212:215], v[108:111]\n\t"
    "s_add_u32 m0, s41, 3424\n\t"
    "v_fma_f32 v92, v92, s40, v249\n\t"
    "v_fma_f32 v93, v93, s40, v249\n\t"
    "v_mfma_f32_16x16x32_bf16 v[108:111], a[24:27], a[216:219], v[108:111]\n\t"
    "v_fma_f32 v94, v94, s40, v249\n\t"
    "v_fma_f32 v95, v95, s40, v249\n\t"
    "v_mfma_f32_16x16x32_bf16 v[108:111], a[28:31], a[220:223], v[108:111]\n\t"
    "v_exp_f32_e32 v92, v92\n\t"
    "v_mfma_f32_16x16x32_bf16 v[112:115], a[0:3], a[224:227], 0\n\t"
    "ds_read_b128 a[184:187], v238 offset:20608\n\t"
    "v_exp_f32_e32 v93, v93\n\t"
    "v_mfma_f32_16x16x32_bf16 v[112:115], a[4:7], a[228:231], v[112:115]\n\t"
    "buffer_load_dwordx4 v240, %2, s63 offen offset:3072 nt lds\n\t"
    "v_exp_f32_e32 v94, v94\n\t"
    "v_mfma_f32_16x16x32_bf16 v[112:115], a[8:11], a[232:235], v[112:115]\n\t"
    "s_add_u32 m0, s41, 70912\n\t"
    "v_exp_f32_e32 v95, v95\n\t"
    "v_cvt_pk_bf16_f32 v44, v92, v93\n\t"
    "v_mfma_f32_16x16x32_bf16 v[112:115], a[12:15], a[236:239], v[112:115]\n\t"
    "v_cvt_pk_bf16_f32 v45, v94, v95\n\t"
    "v_fma_f32 v72, v72, s40, v245\n\t"
    "v_mfma_f32_16x16x32_bf16 v[112:115], a[16:19], a[240:243], v[112:115]\n\t"
    "ds_read_b128 a[188:191], v238 offset:20672\n\t"
    "v_fma_f32 v73, v73, s40, v245\n\t"
    "v_mfma_f32_16x16x32_bf16 v[112:115], a[20:23], a[244:247], v[112:115]\n\t"
    "buffer_load_dwordx4 v240, %2, s64 offen offset:0 nt lds\n\t"
    "v_fma_f32 v74, v74, s40, v245\n\t"
    "v_mfma_f32_16x16x32_bf16 v[112:115], a[24:27], a[248:251], v[112:115]\n\t"
    "v_fma_f32 v75, v75, s40, v245\n\t"
    "v_exp_f32_e32 v72, v72\n\t"
    "v_mfma_f32_16x16x32_bf16 v[112:115], a[28:31], a[252:255], v[112:115]\n\t"
    "s_add_u32 m0, s41, 70944\n\t"
    "v_exp_f32_e32 v73, v73\n\t"
    "v_mfma_f32_16x16x32_bf16 v[228:231], v[232:235], v[20:23], 0\n\t"
    "v_exp_f32_e32 v74, v74\n\t"
    "v_mfma_f32_16x16x32_bf16 v[228:231], v[232:235], v[44:47], v[228:231]\n\t"
    "buffer_load_dwordx4 v240, %2, s64 offen offset:1024 nt lds\n\t"
    "v_exp_f32_e32 v75, v75\n\t"
    "v_mfma_f32_16x16x32_bf16 v[228:231], v[232:235], v[68:71], v[228:231]\n\t"
    "v_cvt_pk_bf16_f32 v76, v72, v73\n\t"
    "v_cvt_pk_bf16_f32 v77, v74, v75\n\t"
    "v_fma_f32 v100, v100, s40, v246\n\t"
    "v_mfma_f32_16x16x32_bf16 v[116:119], a[0:3], a[32:35], 0\n\t"
    "s_add_u32 m0, s41, 70976\n\t"
    "v_fma_f32 v101, v101, s40, v246\n\t"
    "v_fma_f32 v102, v102, s40, v246\n\t"
    "v_mfma_f32_16x16x32_bf16 v[116:119], a[4:7], a[36:39], v[116:119]\n\t"
    "s_waitcnt lgkmcnt(1)\n\t"
    "buffer_load_dwordx4 v240, %2, s64 offen offset:2048 nt lds\n\t"
    "v_fma_f32 v103, v103, s40, v246\n\t"
    "v_mfma_f32_16x16x32_bf16 v[116:119], a[8:11], a[40:43], v[116:119]\n\t"
    "s_add_u32 m0, s41, 71008\n\t"
    "v_exp_f32_e32 v100, v100\n\t"
    "v_mfma_f32_16x16x32_bf16 v[116:119], a[12:15], a[44:47], v[116:119]\n\t"
    "v_exp_f32_e32 v101, v101\n\t"
    "v_mfma_f32_16x16x32_bf16 v[116:119], a[16:19], a[48:51], v[116:119]\n\t"
    "v_exp_f32_e32 v102, v102\n\t"
    "v_mfma_f32_16x16x32_bf16 v[116:119], a[20:23], a[52:55], v[116:119]\n\t"
    "v_exp_f32_e32 v103, v103\n\t"
    "v_mfma_f32_16x16x32_bf16 v[116:119], a[24:27], a[56:59], v[116:119]\n\t"
    "buffer_load_dwordx4 v240, %2, s64 offen offset:3072 nt lds\n\t"
    "v_cvt_pk_bf16_f32 v80, v100, v101\n\t"
    "v_mfma_f32_16x16x32_bf16 v[116:119], a[28:31], a[60:63], v[116:119]\n\t"
    "s_waitcnt vmcnt(12)\n\t"
    "s_barrier\n\t"
    "v_cvt_pk_bf16_f32 v81, v102, v103\n\t"
    "v_fma_f32 v104, v104, s40, v247\n\t"
    "v_mfma_f32_16x16x32_bf16 v[96:99], a[160:163], a[64:67], 0\n\t"
    "PG2_%=:\n\t"
    "s_cbranch_execz PG6_%=\n\t"
    "v_fma_f32 v105, v105, s40, v247\n\t"
    "v_fma_f32 v106, v106, s40, v247\n\t"
    "v_fma_f32 v107, v107, s40, v247\n\t"
    "v_mfma_f32_16x16x32_bf16 v[96:99], a[164:167], a[68:71], v[96:99]\n\t"
    "s_add_u32 m0, s41, 104704\n\t"
    "v_exp_f32_e32 v104, v104\n\t"
    "v_mfma_f32_16x16x32_bf16 v[96:99], a[168:171], a[72:75], v[96:99]\n\t"
    "v_exp_f32_e32 v105, v105\n\t"
    "v_mfma_f32_16x16x32_bf16 v[96:99], a[172:175], a[76:79], v[96:99]\n\t"
    "v_exp_f32_e32 v106, v106\n\t"
    "v_mfma_f32_16x16x32_bf16 v[96:99], a[176:179], a[80:83], v[96:99]\n\t"
    "v_exp_f32_e32 v107, v107\n\t"
    "v_cvt_pk_bf16_f32 v84, v104, v105\n\t"
    "v_mfma_f32_16x16x32_bf16 v[96:99], a[180:183], a[84:87], v[96:99]\n\t"
    "v_cvt_pk_bf16_f32 v85, v106, v107\n\t"
    "v_fma_f32 v108, v108, s40, v248\n\t"
    "v_mfma_f32_16x16x32_bf16 v[96:99], a[184:187], a[88:91], v[96:99]\n\t"
    "s_waitcnt lgkmcnt(0)\n\t"
    "buffer_load_dwordx4 v240, %2, s67 offen offset:0 nt lds\n\t"
    "v_fma_f32 v109, v109, s40, v248\n\t"
    "v_add_u32_e32 v238, 0xffff3a00, v238\n\t"
    "v_mfma_f32_16x16x32_bf16 v[96:99], a[188:191], a[92:95], v[96:99]\n\t"
    "v_fma_f32 v110, v110, s40, v248\n\t"
    "v_fma_f32 v111, v111, s40, v248\n\t"
    "v_mfma_f32_16x16x32_bf16 v[100:103], a[160:163], a[96:99], 0\n\t"
    "s_add_u32 m0, s41, 104736\n\t"
    "v_exp_f32_e32 v108, v108\n\t"
    "v_mfma_f32_16x16x32_bf16 v[100:103], a[164:167], a[100:103], v[100:103]\n\t"
    "ds_read_b128 a[0:3], v238 offset:37120\n\t"
    "v_exp_f32_e32 v109, v109\n\t"
    "v_mfma_f32_16x16x32_bf16 v[100:103], a[168:171], a[104:107], v[100:103]\n\t"
    "v_exp_f32_e32 v110, v110\n\t"
    "v_mfma_f32_16x16x32_bf16 v[100:103], a[172:175], a[108:111], v[100:103]\n\t"
    "v_exp_f32_e32 v111, v111\n\t"
    "v_cvt_pk_bf16_f32 v88, v108, v109\n\t"
    "v_mfma_f32_16x16x32_bf16 v[100:103], a[176:179], a[112:115], v[100:103]\n\t"
    "v_cvt_pk_bf16_f32 v89, v110, v111\n\t"
    "v_fma_f32 v112, v112, s40, v249\n\t"
    "v_mfma_f32_16x16x32_bf16 v[100:103], a[180:183], a[116:119], v[100:103]\n\t"
    "ds_read_b128 a[4:7], v238 offset:37184\n\t"
    "v_fma_f32 v113, v113, s40, v249\n\t"
    "v_mfma_f32_16x16x32_bf16 v[100:103], a[184:187], a[120:123], v[100:103]\n\t"
    "buffer_load_dwordx4 v240, %2, s67 offen offset:1024 nt lds\n\t"
    "v_fma_f32 v114, v114, s40, v249\n\t"
    "v_fma_f32 v115, v115, s40, v249\n\t"
    "v_mfma_f32_16x16x32_bf16 v[100:103], a[188:191], a[124:127], v[100:103]\n\t"
    "v_exp_f32_e32 v112, v112\n\t"
    "v_mfma_f32_16x16x32_bf16 v[104:107], a[160:163], a[128:131], 0\n\t"
    "ds_read_b128 a[8:11], v238 offset:37248\n\t"
    "v_exp_f32_e32 v113, v113\n\t"
    "v_mfma_f32_16x16x32_bf16 v[104:107], a[164:167], a[132:135], v[104:107]\n\t"
    "s_add_u32 m0, s41, 104768\n\t"
    "v_exp_f32_e32 v114, v114\n\t"
    "v_mfma_f32_16x16x32_bf16 v[104:107], a[168:171], a[136:139], v[104:107]\n\t"
    "v_exp_f32_e32 v115, v115\n\t"
    "v_cvt_pk_bf16_f32 v92, v112, v113\n\t"
    "v_mfma_f32_16x16x32_bf16 v[104:107], a[172:175], a[140:143], v[104:107]\n\t"
    "v_cvt_pk_bf16_f32 v93, v114, v115\n\t"
    "v_fma_f32 v116, v116, s40, v244\n\t"
    "v_mfma_f32_16x16x32_bf16 v[104:107], a[176:179], a[144:147], v[104:107]\n\t"
    "ds_read_b128 a[12:15], v238 offset:37312\n\t"
    "v_fma_f32 v117, v117, s40, v244\n\t"
    "v_mfma_f32_16x16x32_bf16 v[104:107], a[180:183], a[148:151], v[104:107]\n\t"
    "v_fma_f32 v118, v118, s40, v244\n\t"
    "v_fma_f32 v119, v119, s40, v244\n\t"
    "v_mfma_f32_16x16x32_bf16 v[104:107], a[184:187], a[152:155], v[104:107]\n\t"
    "buffer_load_dwordx4 v240, %2, s67 offen offset:2048 nt lds\n\t"
    "v_exp_f32_e32 v116, v116\n\t"
    "v_mfma_f32_16x16x32_bf16 v[104:107], a[188:191], a[156:159], v[104:107]\n\t"
    "v_exp_f32_e32 v117, v117\n\t"
    "v_mfma_f32_16x16x32_bf16 v[108:111], a[160:163], a[192:195], 0\n\t"
    "ds_read_b128 a[16:19], v238 offset:37376\n\t"
    "v_exp_f32_e32 v118, v118\n\t"
    "v_mfma_f32_16x16x32_bf16 v[108:111], a[164:167], a[196:199], v[108:111]\n\t"
    "s_add_u32 m0, s41, 104800\n\t"
    "v_exp_f32_e32 v119, v119\n\t"
    "v_mfma_f32_16x16x32_bf16 v[108:111], a[168:171], a[200:203], v[108:111]\n\t"
    "v_cvt_pk_bf16_f32 v72, v116, v117\n\t"
    "v_cvt_pk_bf16_f32 v73, v118, v119\n\t"
    "v_fma_f32 v96, v96, s40, v245\n\t"
    "v_mfma_f32_16x16x32_bf16 v[108:111], a[172:175], a[204:207], v[108:111]\n\t"
    "ds_read_b128 a[20:23], v238 offset:37440\n\t"
    "v_fma_f32 v97, v97, s40, v245\n\t"
    "v_mfma_f32_16x16x32_bf16 v[108:111], a[176:179], a[208:211], v[108:111]\n\t"
    "v_fma_f32 v98, v98, s40, v245\n\t"
    "v_fma_f32 v99, v99, s40, v245\n\t"
    "v_mfma_f32_16x16x32_bf16 v[108:111], a[180:183], a[212:215], v[108:111]\n\t"
    "v_exp_f32_e32 v96, v96\n\t"
    "v_mfma_f32_16x16x32_bf16 v[108:111], a[184:187], a[216:219], v[108:111]\n\t"
    "buffer_load_dwordx4 v240, %2, s67 offen offset:3072 nt lds\n\t"
    "v_exp_f32_e32 v97, v97\n\t"
    "v_mfma_f32_16x16x32_bf16 v[108:111], a[188:191], a[220:223], v[108:111]\n\t"
    "ds_read_b128 a[24:27], v238 offset:37504\n\t"
    "v_exp_f32_e32 v98, v98\n\t"
    "v_mfma_f32_16x16x32_bf16 v[112:115], a[160:163], a[224:227], 0\n\t"
    "s_add_u32 m0, s42, 54016\n\t"
    "v_exp_f32_e32 v99, v99\n\t"
    "v_mfma_f32_16x16x32_bf16 v[112:115], a[164:167], a[228:231], v[112:115]\n\t"
    "v_cvt_pk_bf16_f32 v78, v96, v97\n\t"
    "v_cvt_pk_bf16_f32 v79, v98, v99\n\t"
    "v_mfma_f32_16x16x32_bf16 v[112:115], a[168:171], a[232:235], v[112:115]\n\t"
    "ds_read_b128 a[28:31], v238 offset:37568\n\t"
    "v_fma_f32 v100, v100, s40, v246\n\t"
    "v_fma_f32 v101, v101, s40, v246\n\t"
    "v_mfma_f32_16x16x32_bf16 v[112:115], a[172:175], a[236:239], v[112:115]\n\t"
    "v_fma_f32 v102, v102, s40, v246\n\t"
    "v_fma_f32 v103, v103, s40, v246\n\t"
    "v_mfma_f32_16x16x32_bf16 v[112:115], a[176:179], a[240:243], v[112:115]\n\t"
    "v_exp_f32_e32 v100, v100\n\t"
    "v_mfma_f32_16x16x32_bf16 v[112:115], a[180:183], a[244:247], v[112:115]\n\t"
    "v_exp_f32_e32 v101, v101\n\t"
    "v_mfma_f32_16x16x32_bf16 v[112:115], a[184:187], a[248:251], v[112:115]\n\t"
    "buffer_load_dwordx4 v250, %3, s57 offen offset:0 nt lds\n\t"
    "v_exp_f32_e32 v102, v102\n\t"
    "v_mfma_f32_16x16x32_bf16 v[112:115], a[188:191], a[252:255], v[112:115]\n\t"
    "v_exp_f32_e32 v103, v103\n\t"
    "v_mfma_f32_16x16x32_bf16 v[212:215], v[232:235], v[76:79], v[212:215]\n\t"
    "s_add_u32 m0, s42, 54560\n\t"
    "v_cvt_pk_bf16_f32 v82, v100, v101\n\t"
    "v_cvt_pk_bf16_f32 v83, v102, v103\n\t"
    "v_mfma_f32_16x16x32_bf16 v[116:119], a[160:163], a[32:35], 0\n\t"
    "v_fma_f32 v104, v104, s40, v247\n\t"
    "v_fma_f32 v105, v105, s40, v247\n\t"
    "v_mfma_f32_16x16x32_bf16 v[116:119], a[164:167], a[36:39], v[116:119]\n\t"
    "v_fma_f32 v106, v106, s40, v247\n\t"
    "v_fma_f32 v107, v107, s40, v247\n\t"
    "v_mfma_f32_16x16x32_bf16 v[116:119], a[168:171], a[40:43], v[116:119]\n\t"
    "v_exp_f32_e32 v104, v104\n\t"
    "v_mfma_f32_16x16x32_bf16 v[116:119], a[172:175], a[44:47], v[116:119]\n\t"
    "v_exp_f32_e32 v105, v105\n\t"
    "v_mfma_f32_16x16x32_bf16 v[116:119], a[176:179], a[48:51], v[116:119]\n\t"
    "v_exp_f32_e32 v106, v106\n\t"
    "v_mfma_f32_16x16x32_bf16 v[116:119], a[180:183], a[52:55], v[116:119]\n\t"
    "buffer_load_dwordx4 v250, %3, s57 offen offset:512 nt lds\n\t"
    "v_exp_f32_e32 v107, v107\n\t"
    "v_mfma_f32_16x16x32_bf16 v[116:119], a[184:187], a[56:59], v[116:119]\n\t"
    "v_cvt_pk_bf16_f32 v86, v104, v105\n\t"
    "v_cvt_pk_bf16_f32 v87, v106, v107\n\t"
    "v_mfma_f32_16x16x32_bf16 v[116:119], a[188:191], a[60:63], v[116:119]\n\t"
    "s_waitcnt vmcnt(14) lgkmcnt(0)\n\t"
    "s_barrier\n\t"
    "v_fma_f32 v108, v108, s40, v248\n\t"
    "v_fma_f32 v109, v109, s40, v248\n\t"
    "v_mfma_f32_16x16x32_bf16 v[216:219], v[232:235], v[80:83], v[216:219]\n\t"
    "v_fma_f32 v110, v110, s40, v248\n\t"
    "v_fma_f32 v111, v111, s40, v248\n\t"
    "v_mfma_f32_16x16x32_bf16 v[104:107], a[0:3], a[64:67], 0\n\t"
    "s_add_u32 m0, s41, 87808\n\t"
    "v_exp_f32_e32 v108, v108\n\t"
    "v_mfma_f32_16x16x32_bf16 v[104:107], a[4:7], a[68:71], v[104:107]\n\t"
    "v_exp_f32_e32 v109, v109\n\t"
    "v_mfma_f32_16x16x32_bf16 v[104:107], a[8:11], a[72:75], v[104:107]\n\t"
    "v_exp_f32_e32 v110, v110\n\t"
    "v_mfma_f32_16x16x32_bf16 v[104:107], a[12:15], a[76:79], v[104:107]\n\t"
    "v_exp_f32_e32 v111, v111\n\t"
    "v_cvt_pk_bf16_f32 v90, v108, v109\n\t"
    "v_mfma_f32_16x16x32_bf16 v[104:107], a[16:19], a[80:83], v[104:107]\n\t"
    "v_cvt_pk_bf16_f32 v91, v110, v111\n\t"
    "v_fma_f32 v112, v112, s40, v249\n\t"
    "v_mfma_f32_16x16x32_bf16 v[104:107], a[20:23], a[84:87], v[104:107]\n\t"
    "buffer_load_dwordx4 v240, %2, s65 offen offset:0 nt lds\n\t"
    "v_add_u32_e32 v238, 0xc600, v238\n\t"
    "v_fma_f32 v113, v113, s40, v249\n\t"
    "v_mfma_f32_16x16x32_bf16 v[104:107], a[24:27], a[88:91], v[104:107]\n\t"
    "v_fma_f32 v114, v114, s40, v249\n\t"
    "v_fma_f32 v115, v115, s40, v249\n\t"
    "v_mfma_f32_16x16x32_bf16 v[104:107], a[28:31], a[92:95], v[104:107]\n\t"
    "v_exp_f32_e32 v112, v112\n\t"
    "v_mfma_f32_16x16x32_bf16 v[220:223], v[232:235], v[84:87], v[220:223]\n\t"
    "s_add_u32 m0, s41, 87840\n\t"
    "v_exp_f32_e32 v113, v113\n\t"
    "v_mfma_f32_16x16x32_bf16 v[108:111], a[0:3], a[96:99], 0\n\t"
    "ds_read_b128 a[160:163], v238 offset:37120\n\t"
    "v_exp_f32_e32 v114, v114\n\t"
    "v_mfma_f32_16x16x32_bf16 v[108:111], a[4:7], a[100:103], v[108:111]\n\t"
    "v_exp_f32_e32 v115, v115\n\t"
    "v_mfma_f32_16x16x32_bf16 v[108:111], a[8:11], a[104:107], v[108:111]\n\t"
    "v_cvt_pk_bf16_f32 v94, v112, v113\n\t"
    "v_cvt_pk_bf16_f32 v95, v114, v115\n\t"
    "v_fma_f32 v116, v116, s40, v244\n\t"
    "v_mfma_f32_16x16x32_bf16 v[108:111], a[12:15], a[108:111], v[108:111]\n\t"
    "ds_read_b128 a[164:167], v238 offset:37184\n\t"
    "v_fma_f32 v117, v117, s40, v244\n\t"
    "v_mfma_f32_16x16x32_bf16 v[108:111], a[16:19], a[112:115], v[108:111]\n\t"
    "v_fma_f32 v118, v118, s40, v244\n\t"
    "v_fma_f32 v119, v119, s40, v244\n\t"
    "v_mfma_f32_16x16x32_bf16 v[108:111], a[20:23], a[116:119], v[108:111]\n\t"
    "buffer_load_dwordx4 v240, %2, s65 offen offset:1024 nt lds\n\t"
    "v_mfma_f32_16x16x32_bf16 v[108:111], a[24:27], a[120:123], v[108:111]\n\t"
    "v_exp_f32_e32 v116, v116\n\t"
    "v_mfma_f32_16x16x32_bf16 v[108:111], a[28:31], a[124:127], v[108:111]\n\t"
    "v_exp_f32_e32 v117, v117\n\t"
    "v_mfma_f32_16x16x32_bf16 v[224:227], v[232:235], v[88:91], v[224:227]\n\t"
    "ds_read_b128 a[168:171], v238 offset:37248\n\t"
    "v_exp_f32_e32 v118, v118\n\t"
    "v_mfma_f32_16x16x32_bf16 v[228:231], v[232:235], v[92:95], v[228:231]\n\t"
    "s_add_u32 m0, s41, 87872\n\t"
    "v_exp_f32_e32 v119, v119\n\t"
    "v_cvt_pk_bf16_f32 v74, v116, v117\n\t"
    "v_mfma_f32_16x16x32_bf16 v[112:115], a[0:3], a[128:131], 0\n\t"
    "v_cvt_pk_bf16_f32 v75, v118, v119\n\t"
    "v_fma_f32 v104, v104, s40, v245\n\t"
    "v_mfma_f32_16x16x32_bf16 v[112:115], a[4:7], a[132:135], v[112:115]\n\t"
    "ds_read_b128 a[172:175], v238 offset:37312\n\t"
    "v_fma_f32 v105, v105, s40, v245\n\t"
    "v_mfma_f32_16x16x32_bf16 v[112:115], a[8:11], a[136:139], v[112:115]\n\t"
    "buffer_load_dwordx4 v240, %2, s65 offen offset:2048 nt lds\n\t"
    "v_fma_f32 v106, v106, s40, v245\n\t"
    "v_mfma_f32_16x16x32_bf16 v[112:115], a[12:15], a[140:143], v[112:115]\n\t"
    "ds_read_b128 a[176:179], v238 offset:37376\n\t"
    "v_fma_f32 v107, v107, s40, v245\n\t"
    "v_mfma_f32_16x16x32_bf16 v[112:115], a[16:19], a[144:147], v[112:115]\n\t"
    "v_exp_f32_e32 v104, v104\n\t"
    "v_mfma_f32_16x16x32_bf16 v[112:115], a[20:23], a[148:151], v[112:115]\n\t"
    "v_exp_f32_e32 v105, v105\n\t"
    "v_mfma_f32_16x16x32_bf16 v[112:115], a[24:27], a[152:155], v[112:115]\n\t"
    "v_exp_f32_e32 v106, v106\n\t"
    "v_mfma_f32_16x16x32_bf16 v[112:115], a[28:31], a[156:159], v[112:115]\n\t"
    "s_add_u32 m0, s41, 87904\n\t"
    "v_exp_f32_e32 v107, v107\n\t"
    "v_mfma_f32_16x16x32_bf16 v[208:211], v[232:235], v[72:75], v[208:211]\n\t"
    "v_cvt_pk_bf16_f32 v100, v104, v105\n\t"
    "v_cvt_pk_bf16_f32 v101, v106, v107\n\t"
    "v_mfma_f32_16x16x32_bf16 v[116:119], a[0:3], a[192:195], 0\n\t"
    "ds_read_b128 a[180:183], v238 offset:37440\n\t"
    "v_fma_f32 v108, v108, s40, v246\n\t"
    "v_mfma_f32_16x16x32_bf16 v[116:119], a[4:7], a[196:199], v[116:119]\n\t"
    "v_fma_f32 v109, v109, s40, v246\n\t"
    "v_fma_f32 v110, v110, s40, v246\n\t"
    "v_mfma_f32_16x16x32_bf16 v[116:119], a[8:11], a[200:203], v[116:119]\n\t"
    "buffer_load_dwordx4 v240, %2, s65 offen offset:3072 nt lds\n\t"
    "v_fma_f32 v111, v111, s40, v246\n\t"
    "v_mfma_f32_16x16x32_bf16 v[116:119], a[12:15], a[204:207], v[116:119]\n\t"
    "ds_read_b128 a[184:187], v238 offset:37504\n\t"
    "v_mfma_f32_16x16x32_bf16 v[116:119], a[16:19], a[208:211], v[116:119]\n\t"
    "v_exp_f32_e32 v108, v108\n\t"
    "v_mfma_f32_16x16x32_bf16 v[116:119], a[20:23], a[212:215], v[116:119]\n\t"
    "v_exp_f32_e32 v109, v109\n\t"
    "v_mfma_f32_16x16x32_bf16 v[116:119], a[24:27], a[216:219], v[116:119]\n\t"
    "s_add_u32 m0, s42, 62464\n\t"
    "v_exp_f32_e32 v110, v110\n\t"
    "v_mfma_f32_16x16x32_bf16 v[116:119], a[28:31], a[220:223], v[116:119]\n\t"
    "v_exp_f32_e32 v111, v111\n\t"
    "v_mfma_f32_16x16x32_bf16 v[96:99], a[0:3], a[224:227], 0\n\t"
    "ds_read_b128 a[188:191], v238 offset:37568\n\t"
    "v_cvt_pk_bf16_f32 v104, v108, v109\n\t"
    "v_mfma_f32_16x16x32_bf16 v[96:99], a[4:7], a[228:231], v[96:99]\n\t"
    "v_cvt_pk_bf16_f32 v105, v110, v111\n\t"
    "v_fma_f32 v112, v112, s40, v247\n\t"
    "v_mfma_f32_16x16x32_bf16 v[96:99], a[8:11], a[232:235], v[96:99]\n\t"
    "v_fma_f32 v113, v113, s40, v247\n\t"
    "v_fma_f32 v114, v114, s40, v247\n\t"
    "v_mfma_f32_16x16x32_bf16 v[96:99], a[12:15], a[236:239], v[96:99]\n\t"
    "buffer_load_dwordx4 v250, %3, s59 offen offset:0 nt lds\n\t"
    "v_fma_f32 v115, v115, s40, v247\n\t"
    "v_mfma_f32_16x16x32_bf16 v[96:99], a[16:19], a[240:243], v[96:99]\n\t"
    "v_exp_f32_e32 v112, v112\n\t"
    "v_mfma_f32_16x16x32_bf16 v[96:99], a[20:23], a[244:247], v[96:99]\n\t"
    "v_exp_f32_e32 v113, v113\n\t"
    "v_mfma_f32_16x16x32_bf16 v[96:99], a[24:27], a[248:251], v[96:99]\n\t"
    "v_exp_f32_e32 v114, v114\n\t"
    "v_mfma_f32_16x16x32_bf16 v[96:99], a[28:31], a[252:255], v[96:99]\n\t"
    "s_add_u32 m0, s42, 63008\n\t"
    "v_exp_f32_e32 v115, v115\n\t"
    "v_mfma_f32_16x16x32_bf16 v[124:127], a[0:3], a[32:35], 0\n\t"
    "v_cvt_pk_bf16_f32 v108, v112, v113\n\t"
    "v_cvt_pk_bf16_f32 v109, v114, v115\n\t"
    "v_mfma_f32_16x16x32_bf16 v[124:127], a[4:7], a[36:39], v[124:127]\n\t"
    "v_fma_f32 v116, v116, s40, v248\n\t"
    "v_fma_f32 v117, v117, s40, v248\n\t"
    "v_mfma_f32_16x16x32_bf16 v[124:127], a[8:11], a[40:43], v[124:127]\n\t"
    "v_fma_f32 v118, v118, s40, v248\n\t"
    "v_fma_f32 v119, v119, s40, v248\n\t"
    "v_mfma_f32_16x16x32_bf16 v[124:127], a[12:15], a[44:47], v[124:127]\n\t"
    "buffer_load_dwordx4 v250, %3, s59 offen offset:512 nt lds\n\t"
    "v_mfma_f32_16x16x32_bf16 v[124:127], a[16:19], a[48:51], v[124:127]\n\t"
    "v_exp_f32_e32 v116, v116\n\t"
    "v_mfma_f32_16x16x32_bf16 v[124:127], a[20:23], a[52:55], v[124:127]\n\t"
    "v_exp_f32_e32 v117, v117\n\t"
    "v_mfma_f32_16x16x32_bf16 v[124:127], a[24:27], a[56:59], v[124:127]\n\t"
    "v_exp_f32_e32 v118, v118\n\t"
    "v_mfma_f32_16x16x32_bf16 v[124:127], a[28:31], a[60:63], v[124:127]\n\t"
    "s_waitcnt vmcnt(12) lgkmcnt(0)\n\t"
    "s_barrier\n\t"
    "v_mfma_f32_16x16x32_bf16 v[128:131], a[160:163], a[64:67], 0\n\t"
    "s_add_u32 m0, s41, 138496\n\t"
    "v_exp_f32_e32 v119, v119\n\t"
    "v_mfma_f32_16x16x32_bf16 v[128:131], a[164:167], a[68:71], v[128:131]\n\t"
    "v_cvt_pk_bf16_f32 v112, v116, v117\n\t"
    "v_cvt_pk_bf16_f32 v113, v118, v119\n\t"
    "v_mfma_f32_16x16x32_bf16 v[128:131], a[168:171], a[72:75], v[128:131]\n\t"
    "v_fma_f32 v96, v96, s40, v249\n\t"
    "v_fma_f32 v97, v97, s40, v249\n\t"
    "v_mfma_f32_16x16x32_bf16 v[128:131], a[172:175], a[76:79], v[128:131]\n\t"
    "v_fma_f32 v98, v98, s40, v249\n\t"
    "v_add_u32_e32 v238, 0xfffe7400, v238\n\t"
    "v_mfma_f32_16x16x32_bf16 v[128:131], a[176:179], a[80:83], v[128:131]\n\t"
    "buffer_load_dwordx4 v240, %2, s66 offen offset:0 nt lds\n\t"
    "v_fma_f32 v99, v99, s40, v249\n\t"
    "v_mfma_f32_16x16x32_bf16 v[128:131], a[180:183], a[84:87], v[128:131]\n\t"
    "v_exp_f32_e32 v96, v96\n\t"
    "v_mfma_f32_16x16x32_bf16 v[128:131], a[184:187], a[88:91], v[128:131]\n\t"
    "v_exp_f32_e32 v97, v97\n\t"
    "v_mfma_f32_16x16x32_bf16 v[128:131], a[188:191], a[92:95], v[128:131]\n\t"
    "v_exp_f32_e32 v98, v98\n\t"
    "v_mfma_f32_16x16x32_bf16 v[132:135], a[160:163], a[96:99], 0\n\t"
    "s_add_u32 m0, s41, 138528\n\t"
    "ds_read_b128 a[0:3], v238 offset:3328\n\t"
    "v_mfma_f32_16x16x32_bf16 v[132:135], a[164:167], a[100:103], v[132:135]\n\t"
    "v_exp_f32_e32 v99, v99\n\t"
    "v_mfma_f32_16x16x32_bf16 v[132:135], a[168:171], a[104:107], v[132:135]\n\t"
    "v_cvt_pk_bf16_f32 v116, v96, v97\n\t"
    "v_cvt_pk_bf16_f32 v117, v98, v99\n\t"
    "v_mfma_f32_16x16x32_bf16 v[132:135], a[172:175], a[108:111], v[132:135]\n\t"
    "v_fma_f32 v124, v124, s40, v244\n\t"
    "v_fma_f32 v125, v125, s40, v244\n\t"
    "v_mfma_f32_16x16x32_bf16 v[132:135], a[176:179], a[112:115], v[132:135]\n\t"
    "ds_read_b128 a[4:7], v238 offset:3392\n\t"
    "v_fma_f32 v126, v126, s40, v244\n\t"
    "v_mfma_f32_16x16x32_bf16 v[132:135], a[180:183], a[116:119], v[132:135]\n\t"
    "buffer_load_dwordx4 v240, %2, s66 offen offset:1024 nt lds\n\t"
    "v_fma_f32 v127, v127, s40, v244\n\t"
    "v_mfma_f32_16x16x32_bf16 v[132:135], a[184:187], a[120:123], v[132:135]\n\t"
    "v_exp_f32_e32 v124, v124\n\t"
    "v_mfma_f32_16x16x32_bf16 v[132:135], a[188:191], a[124:127], v[132:135]\n\t"
    "v_exp_f32_e32 v125, v125\n\t"
    "v_mfma_f32_16x16x32_bf16 v[136:139], a[160:163], a[128:131], 0\n\t"
    "s_add_u32 m0, s41, 138560\n\t"
    "ds_read_b128 a[8:11], v238 offset:3456\n\t"
    "v_mfma_f32_16x16x32_bf16 v[136:139], a[164:167], a[132:135], v[136:139]\n\t"
    "v_exp_f32_e32 v126, v126\n\t"
    "v_mfma_f32_16x16x32_bf16 v[136:139], a[168:171], a[136:139], v[136:139]\n\t"
    "v_exp_f32_e32 v127, v127\n\t"
    "v_cvt_pk_bf16_f32 v96, v124, v125\n\t"
    "v_mfma_f32_16x16x32_bf16 v[136:139], a[172:175], a[140:143], v[136:139]\n\t"
    "ds_read_b128 a[12:15], v238 offset:3520\n\t"
    "v_cvt_pk_bf16_f32 v97, v126, v127\n\t"
    "v_mfma_f32_16x16x32_bf16 v[136:139], a[176:179], a[144:147], v[136:139]\n\t"
    "v_fma_f32 v128, v128, s40, v245\n\t"
    "v_fma_f32 v129, v129, s40, v245\n\t"
    "v_mfma_f32_16x16x32_bf16 v[136:139], a[180:183], a[148:151], v[136:139]\n\t"
    "buffer_load_dwordx4 v240, %2, s66 offen offset:2048 nt lds\n\t"
    "v_fma_f32 v130, v130, s40, v245\n\t"
    "v_mfma_f32_16x16x32_bf16 v[136:139], a[184:187], a[152:155], v[136:139]\n\t"
    "ds_read_b128 a[16:19], v238 offset:3584\n\t"
    "v_fma_f32 v131, v131, s40, v245\n\t"
    "v_mfma_f32_16x16x32_bf16 v[136:139], a[188:191], a[156:159], v[136:139]\n\t"
    "v_exp_f32_e32 v128, v128\n\t"
    "v_mfma_f32_16x16x32_bf16 v[124:127], a[160:163], a[192:195], 0\n\t"
    "s_add_u32 m0, s41, 138592\n\t"
    "v_exp_f32_e32 v129, v129\n\t"
    "v_mfma_f32_16x16x32_bf16 v[124:127], a[164:167], a[196:199], v[124:127]\n\t"
    "v_exp_f32_e32 v130, v130\n\t"
    "v_mfma_f32_16x16x32_bf16 v[124:127], a[168:171], a[200:203], v[124:127]\n\t"
    "v_exp_f32_e32 v131, v131\n\t"
    "v_mfma_f32_16x16x32_bf16 v[124:127], a[172:175], a[204:207], v[124:127]\n\t"
    "ds_read_b128 a[20:23], v238 offset:3648\n\t"
    "v_cvt_pk_bf16_f32 v102, v128, v129\n\t"
    "v_mfma_f32_16x16x32_bf16 v[124:127], a[176:179], a[208:211], v[124:127]\n\t"
    "v_cvt_pk_bf16_f32 v103, v130, v131\n\t"
    "v_fma_f32 v132, v132, s40, v246\n\t"
    "v_mfma_f32_16x16x32_bf16 v[124:127], a[180:183], a[212:215], v[124:127]\n\t"
    "buffer_load_dwordx4 v240, %2, s66 offen offset:3072 nt lds\n\t"
    "v_fma_f32 v133, v133, s40, v246\n\t"
    "v_mfma_f32_16x16x32_bf16 v[124:127], a[184:187], a[216:219], v[124:127]\n\t"
    "ds_read_b128 a[24:27], v238 offset:3712\n\t"
    "v_fma_f32 v134, v134, s40, v246\n\t"
    "v_mfma_f32_16x16x32_bf16 v[124:127], a[188:191], a[220:223], v[124:127]\n\t"
    "v_fma_f32 v135, v135, s40, v246\n\t"
    "v_mfma_f32_16x16x32_bf16 v[128:131], a[160:163], a[224:227], 0\n\t"
    "s_add_u32 m0, s42, 20224\n\t"
    "v_exp_f32_e32 v132, v132\n\t"
    "v_mfma_f32_16x16x32_bf16 v[128:131], a[164:167], a[228:231], v[128:131]\n\t"
    "v_exp_f32_e32 v133, v133\n\t"
    "v_mfma_f32_16x16x32_bf16 v[128:131], a[168:171], a[232:235], v[128:131]\n\t"
    "ds_read_b128 a[28:31], v238 offset:3776\n\t"
    "v_exp_f32_e32 v134, v134\n\t"
    "v_mfma_f32_16x16x32_bf16 v[128:131], a[172:175], a[236:239], v[128:131]\n\t"
    "v_exp_f32_e32 v135, v135\n\t"
    "v_cvt_pk_bf16_f32 v106, v132, v133\n\t"
    "v_mfma_f32_16x16x32_bf16 v[128:131], a[176:179], a[240:243], v[128:131]\n\t"
    "v_cvt_pk_bf16_f32 v107, v134, v135\n\t"
    "v_fma_f32 v136, v136, s40, v247\n\t"
    "v_mfma_f32_16x16x32_bf16 v[128:131], a[180:183], a[244:247], v[128:131]\n\t"
    "buffer_load_dwordx4 v250, %3, s61 offen offset:0 nt lds\n\t"
    "v_fma_f32 v137, v137, s40, v247\n\t"
    "v_mfma_f32_16x16x32_bf16 v[128:131], a[184:187], a[248:251], v[128:131]\n\t"
    "v_fma_f32 v138, v138, s40, v247\n\t"
    "v_fma_f32 v139, v139, s40, v247\n\t"
    "v_mfma_f32_16x16x32_bf16 v[128:131], a[188:191], a[252:255], v[128:131]\n\t"
    "v_exp_f32_e32 v136, v136\n\t"
    "v_mfma_f32_16x16x32_bf16 v[212:215], v[232:235], v[100:103], v[212:215]\n\t"
    "s_add_u32 m0, s42, 20768\n\t"
    "v_exp_f32_e32 v137, v137\n\t"
    "v_mfma_f32_16x16x32_bf16 v[132:135], a[160:163], a[32:35], 0\n\t"
    "v_exp_f32_e32 v138, v138\n\t"
    "v_mfma_f32_16x16x32_bf16 v[132:135], a[164:167], a[36:39], v[132:135]\n\t"
    "v_exp_f32_e32 v139, v139\n\t"
    "v_mfma_f32_16x16x32_bf16 v[132:135], a[168:171], a[40:43], v[132:135]\n\t"
    "v_cvt_pk_bf16_f32 v110, v136, v137\n\t"
    "v_cvt_pk_bf16_f32 v111, v138, v139\n\t"
    "v_mfma_f32_16x16x32_bf16 v[132:135], a[172:175], a[44:47], v[132:135]\n\t"
    "v_fma_f32 v124, v124, s40, v248\n\t"
    "v_fma_f32 v125, v125, s40, v248\n\t"
    "v_mfma_f32_16x16x32_bf16 v[132:135], a[176:179], a[48:51], v[132:135]\n\t"
    "buffer_load_dwordx4 v250, %3, s61 offen offset:512 nt lds\n\t"
    "v_fma_f32 v126, v126, s40, v248\n\t"
    "v_mfma_f32_16x16x32_bf16 v[132:135], a[180:183], a[52:55], v[132:135]\n\t"
    "v_fma_f32 v127, v127, s40, v248\n\t"
    "v_mfma_f32_16x16x32_bf16 v[132:135], a[184:187], a[56:59], v[132:135]\n\t"
    "v_exp_f32_e32 v124, v124\n\t"
    "v_mfma_f32_16x16x32_bf16 v[132:135], a[188:191], a[60:63], v[132:135]\n\t"
    "v_exp_f32_e32 v125, v125\n\t"
    "v_mfma_f32_16x16x32_bf16 v[216:219], v[232:235], v[104:107], v[216:219]\n\t"
    "v_exp_f32_e32 v126, v126\n\t"
    "v_mfma_f32_16x16x32_bf16 v[220:223], v[232:235], v[108:111], v[220:223]\n\t"
    "s_add_u32 m0, s41, 121600\n\t"
    "v_exp_f32_e32 v127, v127\n\t"
    "s_waitcnt lgkmcnt(0)\n\t"
    "v_mfma_f32_16x16x32_bf16 v[136:139], a[0:3], a[64:67], 0\n\t"
    "v_cvt_pk_bf16_f32 v114, v124, v125\n\t"
    "v_cvt_pk_bf16_f32 v115, v126, v127\n\t"
    "v_mfma_f32_16x16x32_bf16 v[136:139], a[4:7], a[68:71], v[136:139]\n\t"
    "v_fma_f32 v128, v128, s40, v249\n\t"
    "v_fma_f32 v129, v129, s40, v249\n\t"
    "v_mfma_f32_16x16x32_bf16 v[136:139], a[8:11], a[72:75], v[136:139]\n\t"
    "v_fma_f32 v130, v130, s40, v249\n\t"
    "v_add_u32_e32 v238, 0xc600, v238\n\t"
    "v_mfma_f32_16x16x32_bf16 v[136:139], a[12:15], a[76:79], v[136:139]\n\t"
    "buffer_load_dwordx4 v240, %2, s68 offen offset:0 nt lds\n\t"
    "v_fma_f32 v131, v131, s40, v249\n\t"
    "v_mfma_f32_16x16x32_bf16 v[136:139], a[16:19], a[80:83], v[136:139]\n\t"
    "v_exp_f32_e32 v128, v128\n\t"
    "v_mfma_f32_16x16x32_bf16 v[136:139], a[20:23], a[84:87], v[136:139]\n\t"
    "v_exp_f32_e32 v129, v129\n\t"
    "v_mfma_f32_16x16x32_bf16 v[136:139], a[24:27], a[88:91], v[136:139]\n\t"
    "v_exp_f32_e32 v130, v130\n\t"
    "v_mfma_f32_16x16x32_bf16 v[136:139], a[28:31], a[92:95], v[136:139]\n\t"
    "s_add_u32 m0, s41, 121632\n\t"
    "v_exp_f32_e32 v131, v131\n\t"
    "v_mfma_f32_16x16x32_bf16 v[224:227], v[232:235], v[112:115], v[224:227]\n\t"
    "ds_read_b128 a[160:163], v238 offset:20224\n\t"
    "v_cvt_pk_bf16_f32 v118, v128, v129\n\t"
    "v_mfma_f32_16x16x32_bf16 v[140:143], a[0:3], a[96:99], 0\n\t"
    "v_cvt_pk_bf16_f32 v119, v130, v131\n\t"
    "v_fma_f32 v132, v132, s40, v244\n\t"
    "v_mfma_f32_16x16x32_bf16 v[140:143], a[4:7], a[100:103], v[140:143]\n\t"
    "ds_read_b128 a[164:167], v238 offset:20288\n\t"
    "v_fma_f32 v133, v133, s40, v244\n\t"
    "v_mfma_f32_16x16x32_bf16 v[140:143], a[8:11], a[104:107], v[140:143]\n\t"
    "v_fma_f32 v134, v134, s40, v244\n\t"
    "v_fma_f32 v135, v135, s40, v244\n\t"
    "v_mfma_f32_16x16x32_bf16 v[140:143], a[12:15], a[108:111], v[140:143]\n\t"
    "buffer_load_dwordx4 v240, %2, s68 offen offset:1024 nt lds\n\t"
    "v_mfma_f32_16x16x32_bf16 v[140:143], a[16:19], a[112:115], v[140:143]\n\t"
    "v_exp_f32_e32 v132, v132\n\t"
    "v_mfma_f32_16x16x32_bf16 v[140:143], a[20:23], a[116:119], v[140:143]\n\t"
    "v_exp_f32_e32 v133, v133\n\t"
    "v_mfma_f32_16x16x32_bf16 v[140:143], a[24:27], a[120:123], v[140:143]\n\t"
    "v_exp_f32_e32 v134, v134\n\t"
    "v_mfma_f32_16x16x32_bf16 v[140:143], a[28:31], a[124:127], v[140:143]\n\t"
    "s_add_u32 m0, s41, 121664\n\t"
    "ds_read_b128 a[168:171], v238 offset:20352\n\t"
    "v_mfma_f32_16x16x32_bf16 v[228:231], v[232:235], v[116:119], v[228:231]\n\t"
    "v_exp_f32_e32 v135, v135\n\t"
    "v_cvt_pk_bf16_f32 v98, v132, v133\n\t"
    "v_mfma_f32_16x16x32_bf16 v[120:123], a[0:3], a[128:131], 0\n\t"
    "ds_read_b128 a[172:175], v238 offset:20416\n\t"
    "v_cvt_pk_bf16_f32 v99, v134, v135\n\t"
    "v_mfma_f32_16x16x32_bf16 v[120:123], a[4:7], a[132:135], v[120:123]\n\t"
    "v_fma_f32 v136, v136, s40, v245\n\t"
    "v_fma_f32 v137, v137, s40, v245\n\t"
    "v_mfma_f32_16x16x32_bf16 v[120:123], a[8:11], a[136:139], v[120:123]\n\t"
    "buffer_load_dwordx4 v240, %2, s68 offen offset:2048 nt lds\n\t"
    "v_fma_f32 v138, v138, s40, v245\n\t"
    "v_mfma_f32_16x16x32_bf16 v[120:123], a[12:15], a[140:143], v[120:123]\n\t"
    "ds_read_b128 a[176:179], v238 offset:20480\n\t"
    "v_fma_f32 v139, v139, s40, v245\n\t"
    "v_mfma_f32_16x16x32_bf16 v[120:123], a[16:19], a[144:147], v[120:123]\n\t"
    "v_exp_f32_e32 v136, v136\n\t"
    "v_mfma_f32_16x16x32_bf16 v[120:123], a[20:23], a[148:151], v[120:123]\n\t"
    "v_exp_f32_e32 v137, v137\n\t"
    "v_mfma_f32_16x16x32_bf16 v[120:123], a[24:27], a[152:155], v[120:123]\n\t"
    "v_exp_f32_e32 v138, v138\n\t"
    "v_mfma_f32_16x16x32_bf16 v[120:123], a[28:31], a[156:159], v[120:123]\n\t"
    "s_add_u32 m0, s41, 121696\n\t"
    "v_exp_f32_e32 v139, v139\n\t"
    "v_mfma_f32_16x16x32_bf16 v[208:211], v[232:235], v[96:99], v[208:211]\n\t"
    "ds_read_b128 a[180:183], v238 offset:20544\n\t"
    "v_cvt_pk_bf16_f32 v124, v136, v137\n\t"
    "v_mfma_f32_16x16x32_bf16 v[148:151], a[0:3], a[192:195], 0\n\t"
    "v_cvt_pk_bf16_f32 v125, v138, v139\n\t"
    "v_fma_f32 v140, v140, s40, v246\n\t"
    "v_mfma_f32_16x16x32_bf16 v[148:151], a[4:7], a[196:199], v[148:151]\n\t"
    "v_fma_f32 v141, v141, s40, v246\n\t"
    "v_fma_f32 v142, v142, s40, v246\n\t"
    "v_mfma_f32_16x16x32_bf16 v[148:151], a[8:11], a[200:203], v[148:151]\n\t"
    "buffer_load_dwordx4 v240, %2, s68 offen offset:3072 nt lds\n\t"
    "v_fma_f32 v143, v143, s40, v246\n\t"
    "v_mfma_f32_16x16x32_bf16 v[148:151], a[12:15], a[204:207], v[148:151]\n\t"
    "ds_read_b128 a[184:187], v238 offset:20608\n\t"
    "v_mfma_f32_16x16x32_bf16 v[148:151], a[16:19], a[208:211], v[148:151]\n\t"
    "v_exp_f32_e32 v140, v140\n\t"
    "v_mfma_f32_16x16x32_bf16 v[148:151], a[20:23], a[212:215], v[148:151]\n\t"
    "v_exp_f32_e32 v141, v141\n\t"
    "v_mfma_f32_16x16x32_bf16 v[148:151], a[24:27], a[216:219], v[148:151]\n\t"
    "s_add_u32 m0, s42, 28672\n\t"
    "v_exp_f32_e32 v142, v142\n\t"
    "v_mfma_f32_16x16x32_bf16 v[148:151], a[28:31], a[220:223], v[148:151]\n\t"
    "v_exp_f32_e32 v143, v143\n\t"
    "v_mfma_f32_16x16x32_bf16 v[152:155], a[0:3], a[224:227], 0\n\t"
    "ds_read_b128 a[188:191], v238 offset:20672\n\t"
    "v_cvt_pk_bf16_f32 v128, v140, v141\n\t"
    "v_mfma_f32_16x16x32_bf16 v[152:155], a[4:7], a[228:231], v[152:155]\n\t"
    "v_cvt_pk_bf16_f32 v129, v142, v143\n\t"
    "v_fma_f32 v120, v120, s40, v247\n\t"
    "v_mfma_f32_16x16x32_bf16 v[152:155], a[8:11], a[232:235], v[152:155]\n\t"
    "v_fma_f32 v121, v121, s40, v247\n\t"
    "v_fma_f32 v122, v122, s40, v247\n\t"
    "v_mfma_f32_16x16x32_bf16 v[152:155], a[12:15], a[236:239], v[152:155]\n\t"
    "buffer_load_dwordx4 v250, %3, s63 offen offset:0 nt lds\n\t"
    "v_fma_f32 v123, v123, s40, v247\n\t"
    "v_mfma_f32_16x16x32_bf16 v[152:155], a[16:19], a[240:243], v[152:155]\n\t"
    "v_exp_f32_e32 v120, v120\n\t"
    "v_mfma_f32_16x16x32_bf16 v[152:155], a[20:23], a[244:247], v[152:155]\n\t"
    "v_exp_f32_e32 v121, v121\n\t"
    "v_mfma_f32_16x16x32_bf16 v[152:155], a[24:27], a[248:251], v[152:155]\n\t"
    "v_exp_f32_e32 v122, v122\n\t"
    "v_mfma_f32_16x16x32_bf16 v[152:155], a[28:31], a[252:255], v[152:155]\n\t"
    "s_add_u32 m0, s42, 29216\n\t"
    "v_exp_f32_e32 v123, v123\n\t"
    "v_mfma_f32_16x16x32_bf16 v[156:159], a[0:3], a[32:35], 0\n\t"
    "v_cvt_pk_bf16_f32 v132, v120, v121\n\t"
    "v_cvt_pk_bf16_f32 v133, v122, v123\n\t"
    "v_mfma_f32_16x16x32_bf16 v[156:159], a[4:7], a[36:39], v[156:159]\n\t"
    "v_fma_f32 v148, v148, s40, v248\n\t"
    "v_fma_f32 v149, v149, s40, v248\n\t"
    "v_mfma_f32_16x16x32_bf16 v[156:159], a[8:11], a[40:43], v[156:159]\n\t"
    "v_fma_f32 v150, v150, s40, v248\n\t"
    "v_fma_f32 v151, v151, s40, v248\n\t"
    "v_mfma_f32_16x16x32_bf16 v[156:159], a[12:15], a[44:47], v[156:159]\n\t"
    "buffer_load_dwordx4 v250, %3, s63 offen offset:512 nt lds\n\t"
    "v_mfma_f32_16x16x32_bf16 v[156:159], a[16:19], a[48:51], v[156:159]\n\t"
    "v_exp_f32_e32 v148, v148\n\t"
    "v_mfma_f32_16x16x32_bf16 v[156:159], a[20:23], a[52:55], v[156:159]\n\t"
    "v_exp_f32_e32 v149, v149\n\t"
    "v_mfma_f32_16x16x32_bf16 v[156:159], a[24:27], a[56:59], v[156:159]\n\t"
    "v_exp_f32_e32 v150, v150\n\t"
    "v_mfma_f32_16x16x32_bf16 v[156:159], a[28:31], a[60:63], v[156:159]\n\t"
    "s_waitcnt vmcnt(8) lgkmcnt(0)\n\t"
    "s_barrier\n\t"
    "v_mfma_f32_16x16x32_bf16 v[160:163], a[160:163], a[64:67], 0\n\t"
    "s_add_u32 m0, s42, 155392\n\t"
    "v_exp_f32_e32 v151, v151\n\t"
    "v_mfma_f32_16x16x32_bf16 v[160:163], a[164:167], a[68:71], v[160:163]\n\t"
    "v_cvt_pk_bf16_f32 v136, v148, v149\n\t"
    "v_cvt_pk_bf16_f32 v137, v150, v151\n\t"
    "v_mfma_f32_16x16x32_bf16 v[160:163], a[168:171], a[72:75], v[160:163]\n\t"
    "v_fma_f32 v152, v152, s40, v249\n\t"
    "v_fma_f32 v153, v153, s40, v249\n\t"
    "v_mfma_f32_16x16x32_bf16 v[160:163], a[172:175], a[76:79], v[160:163]\n\t"
    "ds_read_b128 a[0:3], v238 offset:37120\n\t"
    "v_fma_f32 v154, v154, s40, v249\n\t"
    "v_mfma_f32_16x16x32_bf16 v[160:163], a[176:179], a[80:83], v[160:163]\n\t"
    "PG3_%=:\n\t"
    "s_cbranch_execz PG7_%=\n\t"
    "buffer_load_dwordx4 v250, %3, s65 offen offset:0 nt lds\n\t"
    "v_fma_f32 v155, v155, s40, v249\n\t"
    "v_mfma_f32_16x16x32_bf16 v[160:163], a[180:183], a[84:87], v[160:163]\n\t"
    "v_exp_f32_e32 v152, v152\n\t"
    "v_mfma_f32_16x16x32_bf16 v[160:163], a[184:187], a[88:91], v[160:163]\n\t"
    "v_exp_f32_e32 v153, v153\n\t"
    "v_mfma_f32_16x16x32_bf16 v[160:163], a[188:191], a[92:95], v[160:163]\n\t"
    "v_exp_f32_e32 v154, v154\n\t"
    "v_mfma_f32_16x16x32_bf16 v[148:151], a[160:163], a[96:99], 0\n\t"
    "s_add_u32 m0, s42, 155936\n\t"
    "v_exp_f32_e32 v155, v155\n\t"
    "v_mfma_f32_16x16x32_bf16 v[148:151], a[164:167], a[100:103], v[148:151]\n\t"
    "ds_read_b128 a[4:7], v238 offset:37184\n\t"
    "v_cvt_pk_bf16_f32 v140, v152, v153\n\t"
    "v_mfma_f32_16x16x32_bf16 v[148:151], a[168:171], a[104:107], v[148:151]\n\t"
    "v_cvt_pk_bf16_f32 v141, v154, v155\n\t"
    "v_fma_f32 v156, v156, s40, v244\n\t"
    "v_mfma_f32_16x16x32_bf16 v[148:151], a[172:175], a[108:111], v[148:151]\n\t"
    "v_fma_f32 v157, v157, s40, v244\n\t"
    "v_fma_f32 v158, v158, s40, v244\n\t"
    "v_mfma_f32_16x16x32_bf16 v[148:151], a[176:179], a[112:115], v[148:151]\n\t"
    "ds_read_b128 a[8:11], v238 offset:37248\n\t"
    "v_fma_f32 v159, v159, s40, v244\n\t"
    "v_mfma_f32_16x16x32_bf16 v[148:151], a[180:183], a[116:119], v[148:151]\n\t"
    "buffer_load_dwordx4 v250, %3, s65 offen offset:512 nt lds\n\t"
    "v_mfma_f32_16x16x32_bf16 v[148:151], a[184:187], a[120:123], v[148:151]\n\t"
    "v_exp_f32_e32 v156, v156\n\t"
    "v_mfma_f32_16x16x32_bf16 v[148:151], a[188:191], a[124:127], v[148:151]\n\t"
    "v_exp_f32_e32 v157, v157\n\t"
    "v_mfma_f32_16x16x32_bf16 v[152:155], a[160:163], a[128:131], 0\n\t"
    "s_add_u32 m0, s42, 37120\n\t"
    "v_exp_f32_e32 v158, v158\n\t"
    "v_mfma_f32_16x16x32_bf16 v[152:155], a[164:167], a[132:135], v[152:155]\n\t"
    "v_exp_f32_e32 v159, v159\n\t"
    "v_mfma_f32_16x16x32_bf16 v[152:155], a[168:171], a[136:139], v[152:155]\n\t"
    "ds_read_b128 a[12:15], v238 offset:37312\n\t"
    "v_cvt_pk_bf16_f32 v120, v156, v157\n\t"
    "v_mfma_f32_16x16x32_bf16 v[152:155], a[172:175], a[140:143], v[152:155]\n\t"
    "v_cvt_pk_bf16_f32 v121, v158, v159\n\t"
    "v_fma_f32 v160, v160, s40, v245\n\t"
    "v_mfma_f32_16x16x32_bf16 v[152:155], a[176:179], a[144:147], v[152:155]\n\t"
    "ds_read_b128 a[16:19], v238 offset:37376\n\t"
    "v_fma_f32 v161, v161, s40, v245\n\t"
    "v_mfma_f32_16x16x32_bf16 v[152:155], a[180:183], a[148:151], v[152:155]\n\t"
    "buffer_load_dwordx4 v250, %3, s53 offen offset:0 nt lds\n\t"
    "v_fma_f32 v162, v162, s40, v245\n\t"
    "v_mfma_f32_16x16x32_bf16 v[152:155], a[184:187], a[152:155], v[152:155]\n\t"
    "v_fma_f32 v163, v163, s40, v245\n\t"
    "v_mfma_f32_16x16x32_bf16 v[152:155], a[188:191], a[156:159], v[152:155]\n\t"
    "v_exp_f32_e32 v160, v160\n\t"
    "v_mfma_f32_16x16x32_bf16 v[156:159], a[160:163], a[192:195], 0\n\t"
    "s_add_u32 m0, s42, 37664\n\t"
    "v_exp_f32_e32 v161, v161\n\t"
    "v_mfma_f32_16x16x32_bf16 v[156:159], a[164:167], a[196:199], v[156:159]\n\t"
    "v_exp_f32_e32 v162, v162\n\t"
    "v_mfma_f32_16x16x32_bf16 v[156:159], a[168:171], a[200:203], v[156:159]\n\t"
    "ds_read_b128 a[20:23], v238 offset:37440\n\t"
    "v_mfma_f32_16x16x32_bf16 v[156:159], a[172:175], a[204:207], v[156:159]\n\t"
    "v_exp_f32_e32 v163, v163\n\t"
    "v_cvt_pk_bf16_f32 v126, v160, v161\n\t"
    "v_mfma_f32_16x16x32_bf16 v[156:159], a[176:179], a[208:211], v[156:159]\n\t"
    "v_cvt_pk_bf16_f32 v127, v162, v163\n\t"
    "v_fma_f32 v148, v148, s40, v246\n\t"
    "v_mfma_f32_16x16x32_bf16 v[156:159], a[180:183], a[212:215], v[156:159]\n\t"
    "ds_read_b128 a[24:27], v238 offset:37504\n\t"
    "v_fma_f32 v149, v149, s40, v246\n\t"
    "v_mfma_f32_16x16x32_bf16 v[156:159], a[184:187], a[216:219], v[156:159]\n\t"
    "buffer_load_dwordx4 v250, %3, s53 offen offset:512 nt lds\n\t"
    "v_fma_f32 v150, v150, s40, v246\n\t"
    "v_mfma_f32_16x16x32_bf16 v[156:159], a[188:191], a[220:223], v[156:159]\n\t"
    "ds_read_b128 a[28:31], v238 offset:37568\n\t"
    "v_fma_f32 v151, v151, s40, v246\n\t"
    "v_mfma_f32_16x16x32_bf16 v[160:163], a[160:163], a[224:227], 0\n\t"
    "s_add_u32 m0, s42, 45568\n\t"
    "v_exp_f32_e32 v148, v148\n\t"
    "v_mfma_f32_16x16x32_bf16 v[160:163], a[164:167], a[228:231], v[160:163]\n\t"
    "v_exp_f32_e32 v149, v149\n\t"
    "v_mfma_f32_16x16x32_bf16 v[160:163], a[168:171], a[232:235], v[160:163]\n\t"
    "v_exp_f32_e32 v150, v150\n\t"
    "v_mfma_f32_16x16x32_bf16 v[160:163], a[172:175], a[236:239], v[160:163]\n\t"
    "v_exp_f32_e32 v151, v151\n\t"
    "v_cvt_pk_bf16_f32 v130, v148, v149\n\t"
    "v_mfma_f32_16x16x32_bf16 v[160:163], a[176:179], a[240:243], v[160:163]\n\t"
    "v_cvt_pk_bf16_f32 v131, v150, v151\n\t"
    "v_fma_f32 v152, v152, s40, v247\n\t"
    "v_mfma_f32_16x16x32_bf16 v[160:163], a[180:183], a[244:247], v[160:163]\n\t"
    "buffer_load_dwordx4 v250, %3, s55 offen offset:0 nt lds\n\t"
    "v_fma_f32 v153, v153, s40, v247\n\t"
    "v_mfma_f32_16x16x32_bf16 v[160:163], a[184:187], a[248:251], v[160:163]\n\t"
    "v_fma_f32 v154, v154, s40, v247\n\t"
    "v_fma_f32 v155, v155, s40, v247\n\t"
    "v_mfma_f32_16x16x32_bf16 v[160:163], a[188:191], a[252:255], v[160:163]\n\t"
    "v_exp_f32_e32 v152, v152\n\t"
    "v_mfma_f32_16x16x32_bf16 v[212:215], v[232:235], v[124:127], v[212:215]\n\t"
    "s_add_u32 m0, s42, 46112\n\t"
    "v_exp_f32_e32 v153, v153\n\t"
    "v_mfma_f32_16x16x32_bf16 v[148:151], a[160:163], a[32:35], 0\n\t"
    "v_exp_f32_e32 v154, v154\n\t"
    "v_mfma_f32_16x16x32_bf16 v[148:151], a[164:167], a[36:39], v[148:151]\n\t"
    "v_exp_f32_e32 v155, v155\n\t"
    "v_mfma_f32_16x16x32_bf16 v[148:151], a[168:171], a[40:43], v[148:151]\n\t"
    "v_cvt_pk_bf16_f32 v134, v152, v153\n\t"
    "v_cvt_pk_bf16_f32 v135, v154, v155\n\t"
    "v_mfma_f32_16x16x32_bf16 v[148:151], a[172:175], a[44:47], v[148:151]\n\t"
    "v_fma_f32 v156, v156, s40, v248\n\t"
    "v_fma_f32 v157, v157, s40, v248\n\t"
    "v_mfma_f32_16x16x32_bf16 v[148:151], a[176:179], a[48:51], v[148:151]\n\t"
    "buffer_load_dwordx4 v250, %3, s55 offen offset:512 nt lds\n\t"
    "v_fma_f32 v158, v158, s40, v248\n\t"
    "v_mfma_f32_16x16x32_bf16 v[148:151], a[180:183], a[52:55], v[148:151]\n\t"
    "v_fma_f32 v159, v159, s40, v248\n\t"
    "v_mfma_f32_16x16x32_bf16 v[148:151], a[184:187], a[56:59], v[148:151]\n\t"
    "v_exp_f32_e32 v156, v156\n\t"
    "v_mfma_f32_16x16x32_bf16 v[148:151], a[188:191], a[60:63], v[148:151]\n\t"
    "v_exp_f32_e32 v157, v157\n\t"
    "v_mfma_f32_16x16x32_bf16 v[216:219], v[232:235], v[128:131], v[216:219]\n\t"
    "v_exp_f32_e32 v158, v158\n\t"
    "v_mfma_f32_16x16x32_bf16 v[220:223], v[232:235], v[132:135], v[220:223]\n\t"
    "s_add_u32 m0, s42, 3328\n\t"
    "v_exp_f32_e32 v159, v159\n\t"
    "s_waitcnt lgkmcnt(0)\n\t"
    "v_mfma_f32_16x16x32_bf16 v[152:155], a[0:3], a[64:67], 0\n\t"
    "v_cvt_pk_bf16_f32 v138, v156, v157\n\t"
    "v_cvt_pk_bf16_f32 v139, v158, v159\n\t"
    "v_mfma_f32_16x16x32_bf16 v[152:155], a[4:7], a[68:71], v[152:155]\n\t"
    "v_fma_f32 v160, v160, s40, v249\n\t"
    "v_fma_f32 v161, v161, s40, v249\n\t"
    "v_mfma_f32_16x16x32_bf16 v[152:155], a[8:11], a[72:75], v[152:155]\n\t"
    "v_fma_f32 v162, v162, s40, v249\n\t"
    "v_add_u32_e32 v238, 0xc600, v238\n\t"
    "v_mfma_f32_16x16x32_bf16 v[152:155], a[12:15], a[76:79], v[152:155]\n\t"
    "buffer_load_dwordx4 v250, %3, s67 offen offset:0 nt lds\n\t"
    "v_fma_f32 v163, v163, s40, v249\n\t"
    "v_mfma_f32_16x16x32_bf16 v[152:155], a[16:19], a[80:83], v[152:155]\n\t"
    "v_exp_f32_e32 v160, v160\n\t"
    "v_mfma_f32_16x16x32_bf16 v[152:155], a[20:23], a[84:87], v[152:155]\n\t"
    "v_exp_f32_e32 v161, v161\n\t"
    "v_mfma_f32_16x16x32_bf16 v[152:155], a[24:27], a[88:91], v[152:155]\n\t"
    "v_exp_f32_e32 v162, v162\n\t"
    "v_mfma_f32_16x16x32_bf16 v[152:155], a[28:31], a[92:95], v[152:155]\n\t"
    "s_add_u32 m0, s42, 3872\n\t"
    "v_exp_f32_e32 v163, v163\n\t"
    "v_mfma_f32_16x16x32_bf16 v[224:227], v[232:235], v[136:139], v[224:227]\n\t"
    "ds_read_b128 a[160:163], v238 offset:37120\n\t"
    "v_cvt_pk_bf16_f32 v142, v160, v161\n\t"
    "v_mfma_f32_16x16x32_bf16 v[156:159], a[0:3], a[96:99], 0\n\t"
    "v_cvt_pk_bf16_f32 v143, v162, v163\n\t"
    "v_fma_f32 v148, v148, s40, v244\n\t"
    "v_mfma_f32_16x16x32_bf16 v[156:159], a[4:7], a[100:103], v[156:159]\n\t"
    "ds_read_b128 a[164:167], v238 offset:37184\n\t"
    "v_fma_f32 v149, v149, s40, v244\n\t"
    "v_mfma_f32_16x16x32_bf16 v[156:159], a[8:11], a[104:107], v[156:159]\n\t"
    "v_fma_f32 v150, v150, s40, v244\n\t"
    "v_fma_f32 v151, v151, s40, v244\n\t"
    "v_mfma_f32_16x16x32_bf16 v[156:159], a[12:15], a[108:111], v[156:159]\n\t"
    "buffer_load_dwordx4 v250, %3, s67 offen offset:512 nt lds\n\t"
    "v_mfma_f32_16x16x32_bf16 v[156:159], a[16:19], a[112:115], v[156:159]\n\t"
    "v_exp_f32_e32 v148, v148\n\t"
    "v_mfma_f32_16x16x32_bf16 v[156:159], a[20:23], a[116:119], v[156:159]\n\t"
    "v_exp_f32_e32 v149, v149\n\t"
    "v_mfma_f32_16x16x32_bf16 v[156:159], a[24:27], a[120:123], v[156:159]\n\t"
    "v_exp_f32_e32 v150, v150\n\t"
    "v_mfma_f32_16x16x32_bf16 v[156:159], a[28:31], a[124:127], v[156:159]\n\t"
    "s_add_u32 m0, s42, 11648\n\t"
    "ds_read_b128 a[168:171], v238 offset:37248\n\t"
    "v_mfma_f32_16x16x32_bf16 v[228:231], v[232:235], v[140:143], v[228:231]\n\t"
    "v_exp_f32_e32 v151, v151\n\t"
    "v_cvt_pk_bf16_f32 v122, v148, v149\n\t"
    "v_mfma_f32_16x16x32_bf16 v[160:163], a[0:3], a[128:131], 0\n\t"
    "ds_read_b128 a[172:175], v238 offset:37312\n\t"
    "v_cvt_pk_bf16_f32 v123, v150, v151\n\t"
    "v_mfma_f32_16x16x32_bf16 v[160:163], a[4:7], a[132:135], v[160:163]\n\t"
    "v_fma_f32 v152, v152, s40, v245\n\t"
    "v_fma_f32 v153, v153, s40, v245\n\t"
    "v_mfma_f32_16x16x32_bf16 v[160:163], a[8:11], a[136:139], v[160:163]\n\t"
    "buffer_load_dwordx4 v250, %3, s53 offen offset:128 nt lds\n\t"
    "v_fma_f32 v154, v154, s40, v245\n\t"
    "v_mfma_f32_16x16x32_bf16 v[160:163], a[12:15], a[140:143], v[160:163]\n\t"
    "ds_read_b128 a[176:179], v238 offset:37376\n\t"
    "v_fma_f32 v155, v155, s40, v245\n\t"
    "v_mfma_f32_16x16x32_bf16 v[160:163], a[16:19], a[144:147], v[160:163]\n\t"
    "v_exp_f32_e32 v152, v152\n\t"
    "v_mfma_f32_16x16x32_bf16 v[160:163], a[20:23], a[148:151], v[160:163]\n\t"
    "v_exp_f32_e32 v153, v153\n\t"
    "v_mfma_f32_16x16x32_bf16 v[160:163], a[24:27], a[152:155], v[160:163]\n\t"
    "v_exp_f32_e32 v154, v154\n\t"
    "v_mfma_f32_16x16x32_bf16 v[160:163], a[28:31], a[156:159], v[160:163]\n\t"
    "s_add_u32 m0, s42, 12192\n\t"
    "v_exp_f32_e32 v155, v155\n\t"
    "v_mfma_f32_16x16x32_bf16 v[208:211], v[232:235], v[120:123], v[208:211]\n\t"
    "ds_read_b128 a[180:183], v238 offset:37440\n\t"
    "v_cvt_pk_bf16_f32 v148, v152, v153\n\t"
    "v_mfma_f32_16x16x32_bf16 v[164:167], a[0:3], a[192:195], 0\n\t"
    "v_cvt_pk_bf16_f32 v149, v154, v155\n\t"
    "v_fma_f32 v156, v156, s40, v246\n\t"
    "v_mfma_f32_16x16x32_bf16 v[164:167], a[4:7], a[196:199], v[164:167]\n\t"
    "v_fma_f32 v157, v157, s40, v246\n\t"
    "v_fma_f32 v158, v158, s40, v246\n\t"
    "v_mfma_f32_16x16x32_bf16 v[164:167], a[8:11], a[200:203], v[164:167]\n\t"
    "buffer_load_dwordx4 v250, %3, s53 offen offset:640 nt lds\n\t"
    "v_fma_f32 v159, v159, s40, v246\n\t"
    "v_mfma_f32_16x16x32_bf16 v[164:167], a[12:15], a[204:207], v[164:167]\n\t"
    "ds_read_b128 a[184:187], v238 offset:37504\n\t"
    "v_mfma_f32_16x16x32_bf16 v[164:167], a[16:19], a[208:211], v[164:167]\n\t"
    "v_exp_f32_e32 v156, v156\n\t"
    "v_mfma_f32_16x16x32_bf16 v[164:167], a[20:23], a[212:215], v[164:167]\n\t"
    "v_exp_f32_e32 v157, v157\n\t"
    "v_mfma_f32_16x16x32_bf16 v[164:167], a[24:27], a[216:219], v[164:167]\n\t"
    "s_add_u32 m0, s42, 70784\n\t"
    "v_exp_f32_e32 v158, v158\n\t"
    "v_mfma_f32_16x16x32_bf16 v[164:167], a[28:31], a[220:223], v[164:167]\n\t"
    "v_exp_f32_e32 v159, v159\n\t"
    "v_mfma_f32_16x16x32_bf16 v[144:147], a[0:3], a[224:227], 0\n\t"
    "ds_read_b128 a[188:191], v238 offset:37568\n\t"
    "v_cvt_pk_bf16_f32 v152, v156, v157\n\t"
    "v_mfma_f32_16x16x32_bf16 v[144:147], a[4:7], a[228:231], v[144:147]\n\t"
    "v_cvt_pk_bf16_f32 v153, v158, v159\n\t"
    "v_fma_f32 v160, v160, s40, v247\n\t"
    "v_mfma_f32_16x16x32_bf16 v[144:147], a[8:11], a[232:235], v[144:147]\n\t"
    "v_fma_f32 v161, v161, s40, v247\n\t"
    "v_fma_f32 v162, v162, s40, v247\n\t"
    "v_mfma_f32_16x16x32_bf16 v[144:147], a[12:15], a[236:239], v[144:147]\n\t"
    "buffer_load_dwordx4 v250, %3, s55 offen offset:128 nt lds\n\t"
    "v_fma_f32 v163, v163, s40, v247\n\t"
    "v_mfma_f32_16x16x32_bf16 v[144:147], a[16:19], a[240:243], v[144:147]\n\t"
    "v_exp_f32_e32 v160, v160\n\t"
    "v_mfma_f32_16x16x32_bf16 v[144:147], a[20:23], a[244:247], v[144:147]\n\t"
    "v_exp_f32_e32 v161, v161\n\t"
    "v_mfma_f32_16x16x32_bf16 v[144:147], a[24:27], a[248:251], v[144:147]\n\t"
    "v_exp_f32_e32 v162, v162\n\t"
    "v_mfma_f32_16x16x32_bf16 v[144:147], a[28:31], a[252:255], v[144:147]\n\t"
    "s_add_u32 m0, s42, 71328\n\t"
    "v_exp_f32_e32 v163, v163\n\t"
    "v_mfma_f32_16x16x32_bf16 v[172:175], a[0:3], a[32:35], 0\n\t"
    "v_cvt_pk_bf16_f32 v156, v160, v161\n\t"
    "v_cvt_pk_bf16_f32 v157, v162, v163\n\t"
    "v_mfma_f32_16x16x32_bf16 v[172:175], a[4:7], a[36:39], v[172:175]\n\t"
    "v_fma_f32 v164, v164, s40, v248\n\t"
    "v_fma_f32 v165, v165, s40, v248\n\t"
    "v_mfma_f32_16x16x32_bf16 v[172:175], a[8:11], a[40:43], v[172:175]\n\t"
    "v_fma_f32 v166, v166, s40, v248\n\t"
    "v_fma_f32 v167, v167, s40, v248\n\t"
    "v_mfma_f32_16x16x32_bf16 v[172:175], a[12:15], a[44:47], v[172:175]\n\t"
    "buffer_load_dwordx4 v250, %3, s55 offen offset:640 nt lds\n\t"
    "v_mfma_f32_16x16x32_bf16 v[172:175], a[16:19], a[48:51], v[172:175]\n\t"
    "v_exp_f32_e32 v164, v164\n\t"
    "v_mfma_f32_16x16x32_bf16 v[172:175], a[20:23], a[52:55], v[172:175]\n\t"
    "v_exp_f32_e32 v165, v165\n\t"
    "v_mfma_f32_16x16x32_bf16 v[172:175], a[24:27], a[56:59], v[172:175]\n\t"
    "v_exp_f32_e32 v166, v166\n\t"
    "v_mfma_f32_16x16x32_bf16 v[172:175], a[28:31], a[60:63], v[172:175]\n\t"
    "s_waitcnt vmcnt(14) lgkmcnt(0)\n\t"
    "s_barrier\n\t"
    "v_mfma_f32_16x16x32_bf16 v[176:179], a[160:163], a[64:67], 0\n\t"
    "s_add_u32 m0, s42, 79232\n\t"
    "v_exp_f32_e32 v167, v167\n\t"
    "v_mfma_f32_16x16x32_bf16 v[176:179], a[164:167], a[68:71], v[176:179]\n\t"
    "v_cvt_pk_bf16_f32 v160, v164, v165\n\t"
    "v_cvt_pk_bf16_f32 v161, v166, v167\n\t"
    "v_mfma_f32_16x16x32_bf16 v[176:179], a[168:171], a[72:75], v[176:179]\n\t"
    "v_fma_f32 v144, v144, s40, v249\n\t"
    "v_fma_f32 v145, v145, s40, v249\n\t"
    "v_mfma_f32_16x16x32_bf16 v[176:179], a[172:175], a[76:79], v[176:179]\n\t"
    "ds_read_b128 a[0:3], v238 offset:3328\n\t"
    "v_fma_f32 v146, v146, s40, v249\n\t"
    "v_mfma_f32_16x16x32_bf16 v[176:179], a[176:179], a[80:83], v[176:179]\n\t"
    "buffer_load_dwordx4 v250, %3, s57 offen offset:128 nt lds\n\t"
    "v_fma_f32 v147, v147, s40, v249\n\t"
    "v_mfma_f32_16x16x32_bf16 v[176:179], a[180:183], a[84:87], v[176:179]\n\t"
    "v_exp_f32_e32 v144, v144\n\t"
    "v_mfma_f32_16x16x32_bf16 v[176:179], a[184:187], a[88:91], v[176:179]\n\t"
    "v_exp_f32_e32 v145, v145\n\t"
    "v_mfma_f32_16x16x32_bf16 v[176:179], a[188:191], a[92:95], v[176:179]\n\t"
    "v_exp_f32_e32 v146, v146\n\t"
    "v_mfma_f32_16x16x32_bf16 v[180:183], a[160:163], a[96:99], 0\n\t"
    "s_add_u32 m0, s42, 79776\n\t"
    "v_exp_f32_e32 v147, v147\n\t"
    "v_mfma_f32_16x16x32_bf16 v[180:183], a[164:167], a[100:103], v[180:183]\n\t"
    "ds_read_b128 a[4:7], v238 offset:3392\n\t"
    "v_cvt_pk_bf16_f32 v164, v144, v145\n\t"
    "v_mfma_f32_16x16x32_bf16 v[180:183], a[168:171], a[104:107], v[180:183]\n\t"
    "v_cvt_pk_bf16_f32 v165, v146, v147\n\t"
    "v_fma_f32 v172, v172, s40, v244\n\t"
    "v_mfma_f32_16x16x32_bf16 v[180:183], a[172:175], a[108:111], v[180:183]\n\t"
    "v_fma_f32 v173, v173, s40, v244\n\t"
    "v_fma_f32 v174, v174, s40, v244\n\t"
    "v_mfma_f32_16x16x32_bf16 v[180:183], a[176:179], a[112:115], v[180:183]\n\t"
    "ds_read_b128 a[8:11], v238 offset:3456\n\t"
    "v_fma_f32 v175, v175, s40, v244\n\t"
    "v_mfma_f32_16x16x32_bf16 v[180:183], a[180:183], a[116:119], v[180:183]\n\t"
    "buffer_load_dwordx4 v250, %3, s57 offen offset:640 nt lds\n\t"
    "v_mfma_f32_16x16x32_bf16 v[180:183], a[184:187], a[120:123], v[180:183]\n\t"
    "v_exp_f32_e32 v172, v172\n\t"
    "v_mfma_f32_16x16x32_bf16 v[180:183], a[188:191], a[124:127], v[180:183]\n\t"
    "v_exp_f32_e32 v173, v173\n\t"
    "v_mfma_f32_16x16x32_bf16 v[184:187], a[160:163], a[128:131], 0\n\t"
    "s_add_u32 m0, s42, 87680\n\t"
    "v_exp_f32_e32 v174, v174\n\t"
    "v_mfma_f32_16x16x32_bf16 v[184:187], a[164:167], a[132:135], v[184:187]\n\t"
    "v_exp_f32_e32 v175, v175\n\t"
    "v_mfma_f32_16x16x32_bf16 v[184:187], a[168:171], a[136:139], v[184:187]\n\t"
    "ds_read_b128 a[12:15], v238 offset:3520\n\t"
    "v_cvt_pk_bf16_f32 v144, v172, v173\n\t"
    "v_mfma_f32_16x16x32_bf16 v[184:187], a[172:175], a[140:143], v[184:187]\n\t"
    "v_cvt_pk_bf16_f32 v145, v174, v175\n\t"
    "v_fma_f32 v176, v176, s40, v245\n\t"
    "v_mfma_f32_16x16x32_bf16 v[184:187], a[176:179], a[144:147], v[184:187]\n\t"
    "ds_read_b128 a[16:19], v238 offset:3584\n\t"
    "v_fma_f32 v177, v177, s40, v245\n\t"
    "v_mfma_f32_16x16x32_bf16 v[184:187], a[180:183], a[148:151], v[184:187]\n\t"
    "buffer_load_dwordx4 v250, %3, s59 offen offset:128 nt lds\n\t"
    "v_fma_f32 v178, v178, s40, v245\n\t"
    "v_mfma_f32_16x16x32_bf16 v[184:187], a[184:187], a[152:155], v[184:187]\n\t"
    "v_fma_f32 v179, v179, s40, v245\n\t"
    "v_mfma_f32_16x16x32_bf16 v[184:187], a[188:191], a[156:159], v[184:187]\n\t"
    "v_exp_f32_e32 v176, v176\n\t"
    "v_mfma_f32_16x16x32_bf16 v[172:175], a[160:163], a[192:195], 0\n\t"
    "s_add_u32 m0, s42, 88224\n\t"
    "v_exp_f32_e32 v177, v177\n\t"
    "v_mfma_f32_16x16x32_bf16 v[172:175], a[164:167], a[196:199], v[172:175]\n\t"
    "v_exp_f32_e32 v178, v178\n\t"
    "v_mfma_f32_16x16x32_bf16 v[172:175], a[168:171], a[200:203], v[172:175]\n\t"
    "ds_read_b128 a[20:23], v238 offset:3648\n\t"
    "v_mfma_f32_16x16x32_bf16 v[172:175], a[172:175], a[204:207], v[172:175]\n\t"
    "v_exp_f32_e32 v179, v179\n\t"
    "v_cvt_pk_bf16_f32 v150, v176, v177\n\t"
    "v_mfma_f32_16x16x32_bf16 v[172:175], a[176:179], a[208:211], v[172:175]\n\t"
    "v_cvt_pk_bf16_f32 v151, v178, v179\n\t"
    "v_fma_f32 v180, v180, s40, v246\n\t"
    "v_mfma_f32_16x16x32_bf16 v[172:175], a[180:183], a[212:215], v[172:175]\n\t"
    "ds_read_b128 a[24:27], v238 offset:3712\n\t"
    "v_fma_f32 v181, v181, s40, v246\n\t"
    "v_mfma_f32_16x16x32_bf16 v[172:175], a[184:187], a[216:219], v[172:175]\n\t"
    "buffer_load_dwordx4 v250, %3, s59 offen offset:640 nt lds\n\t"
    "v_fma_f32 v182, v182, s40, v246\n\t"
    "v_mfma_f32_16x16x32_bf16 v[172:175], a[188:191], a[220:223], v[172:175]\n\t"
    "ds_read_b128 a[28:31], v238 offset:3776\n\t"
    "v_fma_f32 v183, v183, s40, v246\n\t"
    "v_mfma_f32_16x16x32_bf16 v[176:179], a[160:163], a[224:227], 0\n\t"
    "s_add_u32 m0, s42, 96128\n\t"
    "v_exp_f32_e32 v180, v180\n\t"
    "v_mfma_f32_16x16x32_bf16 v[176:179], a[164:167], a[228:231], v[176:179]\n\t"
    "v_exp_f32_e32 v181, v181\n\t"
    "v_mfma_f32_16x16x32_bf16 v[176:179], a[168:171], a[232:235], v[176:179]\n\t"
    "v_exp_f32_e32 v182, v182\n\t"
    "v_mfma_f32_16x16x32_bf16 v[176:179], a[172:175], a[236:239], v[176:179]\n\t"
    "v_exp_f32_e32 v183, v183\n\t"
    "v_cvt_pk_bf16_f32 v154, v180, v181\n\t"
    "v_mfma_f32_16x16x32_bf16 v[176:179], a[176:179], a[240:243], v[176:179]\n\t"
    "v_cvt_pk_bf16_f32 v155, v182, v183\n\t"
    "v_fma_f32 v184, v184, s40, v247\n\t"
    "v_mfma_f32_16x16x32_bf16 v[176:179], a[180:183], a[244:247], v[176:179]\n\t"
    "buffer_load_dwordx4 v250, %3, s61 offen offset:128 nt lds\n\t"
    "v_fma_f32 v185, v185, s40, v247\n\t"
    "v_mfma_f32_16x16x32_bf16 v[176:179], a[184:187], a[248:251], v[176:179]\n\t"
    "v_fma_f32 v186, v186, s40, v247\n\t"
    "v_fma_f32 v187, v187, s40, v247\n\t"
    "v_mfma_f32_16x16x32_bf16 v[176:179], a[188:191], a[252:255], v[176:179]\n\t"
    "v_exp_f32_e32 v184, v184\n\t"
    "v_mfma_f32_16x16x32_bf16 v[212:215], v[232:235], v[148:151], v[212:215]\n\t"
    "s_add_u32 m0, s42, 96672\n\t"
    "v_exp_f32_e32 v185, v185\n\t"
    "v_mfma_f32_16x16x32_bf16 v[180:183], a[160:163], a[32:35], 0\n\t"
    "v_exp_f32_e32 v186, v186\n\t"
    "v_mfma_f32_16x16x32_bf16 v[180:183], a[164:167], a[36:39], v[180:183]\n\t"
    "v_exp_f32_e32 v187, v187\n\t"
    "v_mfma_f32_16x16x32_bf16 v[180:183], a[168:171], a[40:43], v[180:183]\n\t"
    "v_cvt_pk_bf16_f32 v158, v184, v185\n\t"
    "v_cvt_pk_bf16_f32 v159, v186, v187\n\t"
    "v_mfma_f32_16x16x32_bf16 v[180:183], a[172:175], a[44:47], v[180:183]\n\t"
    "v_fma_f32 v172, v172, s40, v248\n\t"
    "v_fma_f32 v173, v173, s40, v248\n\t"
    "v_mfma_f32_16x16x32_bf16 v[180:183], a[176:179], a[48:51], v[180:183]\n\t"
    "buffer_load_dwordx4 v250, %3, s61 offen offset:640 nt lds\n\t"
    "v_fma_f32 v174, v174, s40, v248\n\t"
    "v_mfma_f32_16x16x32_bf16 v[180:183], a[180:183], a[52:55], v[180:183]\n\t"
    "v_fma_f32 v175, v175, s40, v248\n\t"
    "v_mfma_f32_16x16x32_bf16 v[180:183], a[184:187], a[56:59], v[180:183]\n\t"
    "v_exp_f32_e32 v172, v172\n\t"
    "v_mfma_f32_16x16x32_bf16 v[180:183], a[188:191], a[60:63], v[180:183]\n\t"
    "v_exp_f32_e32 v173, v173\n\t"
    "v_mfma_f32_16x16x32_bf16 v[216:219], v[232:235], v[152:155], v[216:219]\n\t"
    "v_exp_f32_e32 v174, v174\n\t"
    "v_mfma_f32_16x16x32_bf16 v[220:223], v[232:235], v[156:159], v[220:223]\n\t"
    "s_waitcnt lgkmcnt(0)\n\t"
    "v_exp_f32_e32 v175, v175\n\t"
    "v_mfma_f32_16x16x32_bf16 v[184:187], a[0:3], a[64:67], 0\n\t"
    "s_add_u32 m0, s42, 138368\n\t"
    "v_cvt_pk_bf16_f32 v162, v172, v173\n\t"
    "v_cvt_pk_bf16_f32 v163, v174, v175\n\t"
    "v_mfma_f32_16x16x32_bf16 v[184:187], a[4:7], a[68:71], v[184:187]\n\t"
    "v_fma_f32 v176, v176, s40, v249\n\t"
    "v_fma_f32 v177, v177, s40, v249\n\t"
    "v_mfma_f32_16x16x32_bf16 v[184:187], a[8:11], a[72:75], v[184:187]\n\t"
    "ds_read_b128 a[160:163], v238 offset:20224\n\t"
    "v_fma_f32 v178, v178, s40, v249\n\t"
    "v_mfma_f32_16x16x32_bf16 v[184:187], a[12:15], a[76:79], v[184:187]\n\t"
    "v_fma_f32 v179, v179, s40, v249\n\t"
    "v_mfma_f32_16x16x32_bf16 v[184:187], a[16:19], a[80:83], v[184:187]\n\t"
    "v_exp_f32_e32 v176, v176\n\t"
    "v_mfma_f32_16x16x32_bf16 v[184:187], a[20:23], a[84:87], v[184:187]\n\t"
    "v_exp_f32_e32 v177, v177\n\t"
    "v_mfma_f32_16x16x32_bf16 v[184:187], a[24:27], a[88:91], v[184:187]\n\t"
    "v_exp_f32_e32 v178, v178\n\t"
    "v_mfma_f32_16x16x32_bf16 v[184:187], a[28:31], a[92:95], v[184:187]\n\t"
    "v_exp_f32_e32 v179, v179\n\t"
    "v_mfma_f32_16x16x32_bf16 v[224:227], v[232:235], v[160:163], v[224:227]\n\t"
    "buffer_load_dwordx4 v250, %3, s63 offen offset:128 nt lds\n\t"
    "v_cvt_pk_bf16_f32 v166, v176, v177\n\t"
    "v_mfma_f32_16x16x32_bf16 v[188:191], a[0:3], a[96:99], 0\n\t"
    "ds_read_b128 a[164:167], v238 offset:20288\n\t"
    "v_cvt_pk_bf16_f32 v167, v178, v179\n\t"
    "v_mfma_f32_16x16x32_bf16 v[188:191], a[4:7], a[100:103], v[188:191]\n\t"
    "v_fma_f32 v180, v180, s40, v244\n\t"
    "v_fma_f32 v181, v181, s40, v244\n\t"
    "v_mfma_f32_16x16x32_bf16 v[188:191], a[8:11], a[104:107], v[188:191]\n\t"
    "ds_read_b128 a[168:171], v238 offset:20352\n\t"
    "v_fma_f32 v182, v182, s40, v244\n\t"
    "v_mfma_f32_16x16x32_bf16 v[188:191], a[12:15], a[108:111], v[188:191]\n\t"
    "v_fma_f32 v183, v183, s40, v244\n\t"
    "v_mfma_f32_16x16x32_bf16 v[188:191], a[16:19], a[112:115], v[188:191]\n\t"
    "s_add_u32 m0, s42, 138912\n\t"
    "v_exp_f32_e32 v180, v180\n\t"
    "v_mfma_f32_16x16x32_bf16 v[188:191], a[20:23], a[116:119], v[188:191]\n\t"
    "v_exp_f32_e32 v181, v181\n\t"
    "v_mfma_f32_16x16x32_bf16 v[188:191], a[24:27], a[120:123], v[188:191]\n\t"
    "v_exp_f32_e32 v182, v182\n\t"
    "v_mfma_f32_16x16x32_bf16 v[188:191], a[28:31], a[124:127], v[188:191]\n\t"
    "v_exp_f32_e32 v183, v183\n\t"
    "v_mfma_f32_16x16x32_bf16 v[228:231], v[232:235], v[164:167], v[228:231]\n\t"
    "ds_read_b128 a[172:175], v238 offset:20416\n\t"
    "v_cvt_pk_bf16_f32 v146, v180, v181\n\t"
    "v_mfma_f32_16x16x32_bf16 v[168:171], a[0:3], a[128:131], 0\n\t"
    "v_cvt_pk_bf16_f32 v147, v182, v183\n\t"
    "v_fma_f32 v184, v184, s40, v245\n\t"
    "v_mfma_f32_16x16x32_bf16 v[168:171], a[4:7], a[132:135], v[168:171]\n\t"
    "buffer_load_dwordx4 v250, %3, s63 offen offset:640 nt lds\n\t"
    "v_fma_f32 v185, v185, s40, v245\n\t"
    "v_mfma_f32_16x16x32_bf16 v[168:171], a[8:11], a[136:139], v[168:171]\n\t"
    "ds_read_b128 a[176:179], v238 offset:20480\n\t"
    "v_fma_f32 v186, v186, s40, v245\n\t"
    "v_mfma_f32_16x16x32_bf16 v[168:171], a[12:15], a[140:143], v[168:171]\n\t"
    "v_fma_f32 v187, v187, s40, v245\n\t"
    "v_mfma_f32_16x16x32_bf16 v[168:171], a[16:19], a[144:147], v[168:171]\n\t"
    "v_exp_f32_e32 v184, v184\n\t"
    "v_mfma_f32_16x16x32_bf16 v[168:171], a[20:23], a[148:151], v[168:171]\n\t"
    "v_exp_f32_e32 v185, v185\n\t"
    "v_mfma_f32_16x16x32_bf16 v[168:171], a[24:27], a[152:155], v[168:171]\n\t"
    "v_exp_f32_e32 v186, v186\n\t"
    "v_mfma_f32_16x16x32_bf16 v[168:171], a[28:31], a[156:159], v[168:171]\n\t"
    "v_exp_f32_e32 v187, v187\n\t"
    "v_mfma_f32_16x16x32_bf16 v[208:211], v[232:235], v[144:147], v[208:211]\n\t"
    "s_add_u32 m0, s42, 146816\n\t"
    "ds_read_b128 a[180:183], v238 offset:20544\n\t"
    "v_cvt_pk_bf16_f32 v172, v184, v185\n\t"
    "v_mfma_f32_16x16x32_bf16 v[192:195], a[0:3], a[192:195], 0\n\t"
    "v_cvt_pk_bf16_f32 v173, v186, v187\n\t"
    "v_fma_f32 v188, v188, s40, v246\n\t"
    "v_mfma_f32_16x16x32_bf16 v[192:195], a[4:7], a[196:199], v[192:195]\n\t"
    "v_fma_f32 v189, v189, s40, v246\n\t"
    "v_fma_f32 v190, v190, s40, v246\n\t"
    "v_mfma_f32_16x16x32_bf16 v[192:195], a[8:11], a[200:203], v[192:195]\n\t"
    "ds_read_b128 a[184:187], v238 offset:20608\n\t"
    "v_fma_f32 v191, v191, s40, v246\n\t"
    "v_mfma_f32_16x16x32_bf16 v[192:195], a[12:15], a[204:207], v[192:195]\n\t"
    "v_exp_f32_e32 v188, v188\n\t"
    "v_mfma_f32_16x16x32_bf16 v[192:195], a[16:19], a[208:211], v[192:195]\n\t"
    "v_exp_f32_e32 v189, v189\n\t"
    "v_mfma_f32_16x16x32_bf16 v[192:195], a[20:23], a[212:215], v[192:195]\n\t"
    "buffer_load_dwordx4 v250, %3, s65 offen offset:128 nt lds\n\t"
    "v_mfma_f32_16x16x32_bf16 v[192:195], a[24:27], a[216:219], v[192:195]\n\t"
    "v_exp_f32_e32 v190, v190\n\t"
    "v_mfma_f32_16x16x32_bf16 v[192:195], a[28:31], a[220:223], v[192:195]\n\t"
    "v_exp_f32_e32 v191, v191\n\t"
    "v_mfma_f32_16x16x32_bf16 v[196:199], a[0:3], a[224:227], 0\n\t"
    "ds_read_b128 a[188:191], v238 offset:20672\n\t"
    "v_cvt_pk_bf16_f32 v176, v188, v189\n\t"
    "v_mfma_f32_16x16x32_bf16 v[196:199], a[4:7], a[228:231], v[196:199]\n\t"
    "v_cvt_pk_bf16_f32 v177, v190, v191\n\t"
    "v_fma_f32 v168, v168, s40, v247\n\t"
    "v_mfma_f32_16x16x32_bf16 v[196:199], a[8:11], a[232:235], v[196:199]\n\t"
    "v_fma_f32 v169, v169, s40, v247\n\t"
    "v_fma_f32 v170, v170, s40, v247\n\t"
    "v_mfma_f32_16x16x32_bf16 v[196:199], a[12:15], a[236:239], v[196:199]\n\t"
    "v_fma_f32 v171, v171, s40, v247\n\t"
    "v_mfma_f32_16x16x32_bf16 v[196:199], a[16:19], a[240:243], v[196:199]\n\t"
    "s_add_u32 m0, s42, 147360\n\t"
    "v_exp_f32_e32 v168, v168\n\t"
    "v_mfma_f32_16x16x32_bf16 v[196:199], a[20:23], a[244:247], v[196:199]\n\t"
    "v_exp_f32_e32 v169, v169\n\t"
    "v_mfma_f32_16x16x32_bf16 v[196:199], a[24:27], a[248:251], v[196:199]\n\t"
    "v_exp_f32_e32 v170, v170\n\t"
    "v_mfma_f32_16x16x32_bf16 v[196:199], a[28:31], a[252:255], v[196:199]\n\t"
    "v_exp_f32_e32 v171, v171\n\t"
    "v_mfma_f32_16x16x32_bf16 v[200:203], a[0:3], a[32:35], 0\n\t"
    "v_cvt_pk_bf16_f32 v180, v168, v169\n\t"
    "v_cvt_pk_bf16_f32 v181, v170, v171\n\t"
    "v_mfma_f32_16x16x32_bf16 v[200:203], a[4:7], a[36:39], v[200:203]\n\t"
    "v_fma_f32 v192, v192, s40, v248\n\t"
    "v_fma_f32 v193, v193, s40, v248\n\t"
    "v_mfma_f32_16x16x32_bf16 v[200:203], a[8:11], a[40:43], v[200:203]\n\t"
    "buffer_load_dwordx4 v250, %3, s65 offen offset:640 nt lds\n\t"
    "v_fma_f32 v194, v194, s40, v248\n\t"
    "v_mfma_f32_16x16x32_bf16 v[200:203], a[12:15], a[44:47], v[200:203]\n\t"
    "v_fma_f32 v195, v195, s40, v248\n\t"
    "v_mfma_f32_16x16x32_bf16 v[200:203], a[16:19], a[48:51], v[200:203]\n\t"
    "v_exp_f32_e32 v192, v192\n\t"
    "v_mfma_f32_16x16x32_bf16 v[200:203], a[20:23], a[52:55], v[200:203]\n\t"
    "v_exp_f32_e32 v193, v193\n\t"
    "v_mfma_f32_16x16x32_bf16 v[200:203], a[24:27], a[56:59], v[200:203]\n\t"
    "v_exp_f32_e32 v194, v194\n\t"
    "v_mfma_f32_16x16x32_bf16 v[200:203], a[28:31], a[60:63], v[200:203]\n\t"
    "s_waitcnt vmcnt(14) lgkmcnt(0)\n\t"
    "s_barrier\n\t"
    "v_mfma_f32_16x16x32_bf16 v[204:207], a[160:163], a[32:35], 0\n\t"
    "v_exp_f32_e32 v195, v195\n\t"
    "v_mfma_f32_16x16x32_bf16 v[204:207], a[164:167], a[36:39], v[204:207]\n\t"
    "ds_read_b64_tr_b16 a[0:1], v251 offset:37120\n\t"
    "v_cvt_pk_bf16_f32 v184, v192, v193\n\t"
    "v_mfma_f32_16x16x32_bf16 v[204:207], a[168:171], a[40:43], v[204:207]\n\t"
    "ds_read_b64_tr_b16 a[2:3], v251 offset:37632\n\t"
    "v_cvt_pk_bf16_f32 v185, v194, v195\n\t"
    "v_mfma_f32_16x16x32_bf16 v[204:207], a[172:175], a[44:47], v[204:207]\n\t"
    "ds_read_b64_tr_b16 a[4:5], v251 offset:45568\n\t"
    "v_fma_f32 v196, v196, s40, v249\n\t"
    "v_mfma_f32_16x16x32_bf16 v[204:207], a[176:179], a[48:51], v[204:207]\n\t"
    "s_add_u32 m0, s42, 129920\n\t"
    "v_fma_f32 v197, v197, s40, v249\n\t"
    "v_fma_f32 v198, v198, s40, v249\n\t"
    "v_mfma_f32_16x16x32_bf16 v[204:207], a[180:183], a[52:55], v[204:207]\n\t"
    "ds_read_b64_tr_b16 a[6:7], v251 offset:46080\n\t"
    "v_fma_f32 v199, v199, s40, v249\n\t"
    "v_mfma_f32_16x16x32_bf16 v[204:207], a[184:187], a[56:59], v[204:207]\n\t"
    "v_add_u32_e32 v251, 0xc600, v251\n\t"
    "v_mfma_f32_16x16x32_bf16 v[204:207], a[188:191], a[60:63], v[204:207]\n\t"
    "v_exp_f32_e32 v196, v196\n\t"
    "v_mfma_f32_16x16x32_bf16 v[192:195], a[160:163], a[64:67], 0\n\t"
    "ds_read_b64_tr_b16 a[8:9], v251 offset:3328\n\t"
    "v_exp_f32_e32 v197, v197\n\t"
    "v_mfma_f32_16x16x32_bf16 v[192:195], a[164:167], a[68:71], v[192:195]\n\t"
    "ds_read_b64_tr_b16 a[10:11], v251 offset:3840\n\t"
    "v_exp_f32_e32 v198, v198\n\t"
    "v_mfma_f32_16x16x32_bf16 v[192:195], a[168:171], a[72:75], v[192:195]\n\t"
    "ds_read_b64_tr_b16 a[12:13], v251 offset:11776\n\t"
    "v_exp_f32_e32 v199, v199\n\t"
    "v_mfma_f32_16x16x32_bf16 v[192:195], a[172:175], a[76:79], v[192:195]\n\t"
    "ds_read_b64_tr_b16 a[14:15], v251 offset:12288\n\t"
    "v_cvt_pk_bf16_f32 v188, v196, v197\n\t"
    "v_cvt_pk_bf16_f32 v189, v198, v199\n\t"
    "v_mfma_f32_16x16x32_bf16 v[192:195], a[176:179], a[80:83], v[192:195]\n\t"
    "v_fma_f32 v200, v200, s40, v244\n\t"
    "v_add_u32_e32 v251, 0xffff3a00, v251\n\t"
    "v_mfma_f32_16x16x32_bf16 v[192:195], a[180:183], a[84:87], v[192:195]\n\t"
    "ds_read_b64_tr_b16 a[16:17], v251 offset:20224\n\t"
    "v_fma_f32 v201, v201, s40, v244\n\t"
    "v_mfma_f32_16x16x32_bf16 v[192:195], a[184:187], a[88:91], v[192:195]\n\t"
    "v_fma_f32 v202, v202, s40, v244\n\t"
    "v_fma_f32 v203, v203, s40, v244\n\t"
    "v_mfma_f32_16x16x32_bf16 v[192:195], a[188:191], a[92:95], v[192:195]\n\t"
    "ds_read_b64_tr_b16 a[18:19], v251 offset:20736\n\t"
    "v_exp_f32_e32 v200, v200\n\t"
    "v_mfma_f32_16x16x32_bf16 v[196:199], a[160:163], a[96:99], 0\n\t"
    "ds_read_b64_tr_b16 a[20:21], v251 offset:28672\n\t"
    "v_exp_f32_e32 v201, v201\n\t"
    "v_mfma_f32_16x16x32_bf16 v[196:199], a[164:167], a[100:103], v[196:199]\n\t"
    "PG4_%=:\n\t"
    "s_cbranch_execz RET_%=\n\t"
    "ds_read_b64_tr_b16 a[22:23], v251 offset:29184\n\t"
    "v_exp_f32_e32 v202, v202\n\t"
    "v_mfma_f32_16x16x32_bf16 v[196:199], a[168:171], a[104:107], v[196:199]\n\t"
    "buffer_load_dwordx4 v250, %3, s67 offen offset:128 nt lds\n\t"
    "v_exp_f32_e32 v203, v203\n\t"
    "v_mfma_f32_16x16x32_bf16 v[196:199], a[172:175], a[108:111], v[196:199]\n\t"
    "v_add_u32_e32 v251, 0x25200, v251\n\t"
    "v_cvt_pk_bf16_f32 v168, v200, v201\n\t"
    "v_cvt_pk_bf16_f32 v169, v202, v203\n\t"
    "v_mfma_f32_16x16x32_bf16 v[196:199], a[176:179], a[112:115], v[196:199]\n\t"
    "ds_read_b64_tr_b16 a[24:25], v251 offset:3328\n\t"
    "v_fma_f32 v204, v204, s40, v244\n\t"
    "v_mfma_f32_16x16x32_bf16 v[196:199], a[180:183], a[116:119], v[196:199]\n\t"
    "ds_read_b64_tr_b16 a[26:27], v251 offset:3840\n\t"
    "v_add_u32_e32 v251, 0xfffdae00, v251\n\t"
    "v_mfma_f32_16x16x32_bf16 v[196:199], a[184:187], a[120:123], v[196:199]\n\t"
    "ds_read_b64_tr_b16 a[28:29], v251 offset:3328\n\t"
    "v_fma_f32 v205, v205, s40, v244\n\t"
    "v_mfma_f32_16x16x32_bf16 v[196:199], a[188:191], a[124:127], v[196:199]\n\t"
    "ds_read_b64_tr_b16 a[30:31], v251 offset:3840\n\t"
    "v_fma_f32 v206, v206, s40, v244\n\t"
    "v_fma_f32 v207, v207, s40, v244\n\t"
    "v_mfma_f32_16x16x32_bf16 v[200:203], a[160:163], a[128:131], 0\n\t"
    "v_exp_f32_e32 v204, v204\n\t"
    "v_mfma_f32_16x16x32_bf16 v[200:203], a[164:167], a[132:135], v[200:203]\n\t"
    "ds_read_b64_tr_b16 a[32:33], v251 offset:37152\n\t"
    "v_exp_f32_e32 v205, v205\n\t"
    "v_mfma_f32_16x16x32_bf16 v[200:203], a[168:171], a[136:139], v[200:203]\n\t"
    "v_exp_f32_e32 v206, v206\n\t"
    "v_mfma_f32_16x16x32_bf16 v[200:203], a[172:175], a[140:143], v[200:203]\n\t"
    "ds_read_b64_tr_b16 a[34:35], v251 offset:37664\n\t"
    "v_exp_f32_e32 v207, v207\n\t"
    "v_mfma_f32_16x16x32_bf16 v[200:203], a[176:179], a[144:147], v[200:203]\n\t"
    "ds_read_b64_tr_b16 a[36:37], v251 offset:45600\n\t"
    "v_cvt_pk_bf16_f32 v170, v204, v205\n\t"
    "v_cvt_pk_bf16_f32 v171, v206, v207\n\t"
    "v_mfma_f32_16x16x32_bf16 v[200:203], a[180:183], a[148:151], v[200:203]\n\t"
    "s_add_u32 m0, s42, 130464\n\t"
    "v_fma_f32 v192, v192, s40, v245\n\t"
    "v_fma_f32 v193, v193, s40, v245\n\t"
    "v_mfma_f32_16x16x32_bf16 v[200:203], a[184:187], a[152:155], v[200:203]\n\t"
    "ds_read_b64_tr_b16 a[38:39], v251 offset:46112\n\t"
    "v_fma_f32 v194, v194, s40, v245\n\t"
    "v_mfma_f32_16x16x32_bf16 v[200:203], a[188:191], a[156:159], v[200:203]\n\t"
    "v_fma_f32 v195, v195, s40, v245\n\t"
    "v_add_u32_e32 v251, 0xc600, v251\n\t"
    "v_mfma_f32_16x16x32_bf16 v[204:207], a[160:163], a[192:195], 0\n\t"
    "v_exp_f32_e32 v192, v192\n\t"
    "v_mfma_f32_16x16x32_bf16 v[204:207], a[164:167], a[196:199], v[204:207]\n\t"
    "v_exp_f32_e32 v193, v193\n\t"
    "v_mfma_f32_16x16x32_bf16 v[204:207], a[168:171], a[200:203], v[204:207]\n\t"
    "ds_read_b64_tr_b16 a[40:41], v251 offset:3360\n\t"
    "v_exp_f32_e32 v194, v194\n\t"
    "v_mfma_f32_16x16x32_bf16 v[204:207], a[172:175], a[204:207], v[204:207]\n\t"
    "v_exp_f32_e32 v195, v195\n\t"
    "v_cvt_pk_bf16_f32 v174, v192, v193\n\t"
    "v_mfma_f32_16x16x32_bf16 v[204:207], a[176:179], a[208:211], v[204:207]\n\t"
    "ds_read_b64_tr_b16 a[42:43], v251 offset:3872\n\t"
    "v_cvt_pk_bf16_f32 v175, v194, v195\n\t"
    "v_mfma_f32_16x16x32_bf16 v[204:207], a[180:183], a[212:215], v[204:207]\n\t"
    "ds_read_b64_tr_b16 a[44:45], v251 offset:11808\n\t"
    "v_fma_f32 v196, v196, s40, v246\n\t"
    "v_mfma_f32_16x16x32_bf16 v[204:207], a[184:187], a[216:219], v[204:207]\n\t"
    "v_fma_f32 v197, v197, s40, v246\n\t"
    "v_fma_f32 v198, v198, s40, v246\n\t"
    "v_mfma_f32_16x16x32_bf16 v[204:207], a[188:191], a[220:223], v[204:207]\n\t"
    "ds_read_b64_tr_b16 a[46:47], v251 offset:12320\n\t"
    "v_fma_f32 v199, v199, s40, v246\n\t"
    "v_mfma_f32_16x16x32_bf16 v[208:211], v[232:235], v[168:171], v[208:211]\n\t"
    "v_exp_f32_e32 v196, v196\n\t"
    "v_mfma_f32_16x16x32_bf16 v[192:195], a[160:163], a[224:227], 0\n\t"
    "v_exp_f32_e32 v197, v197\n\t"
    "v_add_u32_e32 v251, 0xffff3a00, v251\n\t"
    "v_mfma_f32_16x16x32_bf16 v[192:195], a[164:167], a[228:231], v[192:195]\n\t"
    "buffer_load_dwordx4 v250, %3, s67 offen offset:640 nt lds\n\t"
    "v_exp_f32_e32 v198, v198\n\t"
    "v_mfma_f32_16x16x32_bf16 v[192:195], a[168:171], a[232:235], v[192:195]\n\t"
    "ds_read_b64_tr_b16 a[48:49], v251 offset:20256\n\t"
    "v_exp_f32_e32 v199, v199\n\t"
    "v_mfma_f32_16x16x32_bf16 v[192:195], a[172:175], a[236:239], v[192:195]\n\t"
    "v_cvt_pk_bf16_f32 v178, v196, v197\n\t"
    "v_cvt_pk_bf16_f32 v179, v198, v199\n\t"
    "v_mfma_f32_16x16x32_bf16 v[192:195], a[176:179], a[240:243], v[192:195]\n\t"
    "ds_read_b64_tr_b16 a[50:51], v251 offset:20768\n\t"
    "v_fma_f32 v200, v200, s40, v247\n\t"
    "v_mfma_f32_16x16x32_bf16 v[192:195], a[180:183], a[244:247], v[192:195]\n\t"
    "ds_read_b64_tr_b16 a[52:53], v251 offset:28704\n\t"
    "v_fma_f32 v201, v201, s40, v247\n\t"
    "v_mfma_f32_16x16x32_bf16 v[192:195], a[184:187], a[248:251], v[192:195]\n\t"
    "v_fma_f32 v202, v202, s40, v247\n\t"
    "v_fma_f32 v203, v203, s40, v247\n\t"
    "v_mfma_f32_16x16x32_bf16 v[192:195], a[188:191], a[252:255], v[192:195]\n\t"
    "ds_read_b64_tr_b16 a[54:55], v251 offset:29216\n\t"
    "v_exp_f32_e32 v200, v200\n\t"
    "v_mfma_f32_16x16x32_bf16 v[212:215], v[232:235], v[172:175], v[212:215]\n\t"
    "v_exp_f32_e32 v201, v201\n\t"
    "v_mfma_f32_16x16x32_bf16 v[216:219], v[232:235], v[176:179], v[216:219]\n\t"
    "s_waitcnt lgkmcnt(4)\n\t"
    "v_exp_f32_e32 v202, v202\n\t"
    "v_add_u32_e32 v251, 0x25200, v251\n\t"
    "v_mfma_f32_16x16x32_bf16 v[196:199], a[0:3], v[0:3], 0\n\t"
    "v_exp_f32_e32 v203, v203\n\t"
    "v_cvt_pk_bf16_f32 v182, v200, v201\n\t"
    "v_mfma_f32_16x16x32_bf16 v[196:199], a[4:7], v[24:27], v[196:199]\n\t"
    "ds_read_b64_tr_b16 a[56:57], v251 offset:3360\n\t"
    "v_cvt_pk_bf16_f32 v183, v202, v203\n\t"
    "v_mfma_f32_16x16x32_bf16 v[196:199], a[8:11], v[48:51], v[196:199]\n\t"
    "ds_read_b64_tr_b16 a[58:59], v251 offset:3872\n\t"
    "v_fma_f32 v204, v204, s40, v248\n\t"
    "v_mfma_f32_16x16x32_bf16 v[196:199], a[12:15], v[72:75], v[196:199]\n\t"
    "v_fma_f32 v205, v205, s40, v248\n\t"
    "v_fma_f32 v206, v206, s40, v248\n\t"
    "v_mfma_f32_16x16x32_bf16 v[196:199], a[16:19], v[96:99], v[196:199]\n\t"
    "buffer_load_dword v241, v237, %3, s37 offen \n\t"
    "v_fma_f32 v207, v207, s40, v248\n\t"
    "v_add_u32_e32 v251, 0xfffdae00, v251\n\t"
    "v_mfma_f32_16x16x32_bf16 v[196:199], a[20:23], v[120:123], v[196:199]\n\t"
    "v_exp_f32_e32 v204, v204\n\t"
    "v_mfma_f32_16x16x32_bf16 v[196:199], a[24:27], v[144:147], v[196:199]\n\t"
    "ds_read_b64_tr_b16 a[60:61], v251 offset:3360\n\t"
    "v_exp_f32_e32 v205, v205\n\t"
    "v_mfma_f32_16x16x32_bf16 v[196:199], a[28:31], v[168:171], v[196:199]\n\t"
    "v_exp_f32_e32 v206, v206\n\t"
    "v_mfma_f32_16x16x32_bf16 v[220:223], v[232:235], v[180:183], v[220:223]\n\t"
    "ds_read_b64_tr_b16 a[62:63], v251 offset:3872\n\t"
    "v_exp_f32_e32 v207, v207\n\t"
    "v_mfma_f32_16x16x32_bf16 v[200:203], a[0:3], v[4:7], 0\n\t"
    "v_cvt_pk_bf16_f32 v186, v204, v205\n\t"
    "v_cvt_pk_bf16_f32 v187, v206, v207\n\t"
    "v_mfma_f32_16x16x32_bf16 v[200:203], a[4:7], v[28:31], v[200:203]\n\t"
    "v_fma_f32 v192, v192, s40, v249\n\t"
    "v_fma_f32 v193, v193, s40, v249\n\t"
    "v_mfma_f32_16x16x32_bf16 v[200:203], a[8:11], v[52:55], v[200:203]\n\t"
    "ds_read_b64_tr_b16 a[64:65], v252 offset:37120\n\t"
    "v_fma_f32 v194, v194, s40, v249\n\t"
    "v_mfma_f32_16x16x32_bf16 v[200:203], a[12:15], v[76:79], v[200:203]\n\t"
    "ds_write_b64 v253, v[196:197] offset:59136\n\t"
    "v_mfma_f32_16x16x32_bf16 v[200:203], a[16:19], v[100:103], v[200:203]\n\t"
    "v_fma_f32 v195, v195, s40, v249\n\t"
    "v_mfma_f32_16x16x32_bf16 v[200:203], a[20:23], v[124:127], v[200:203]\n\t"
    "ds_write_b64 v253, v[198:199] offset:59144\n\t"
    "v_mfma_f32_16x16x32_bf16 v[200:203], a[24:27], v[148:151], v[200:203]\n\t"
    "v_exp_f32_e32 v192, v192\n\t"
    "v_mfma_f32_16x16x32_bf16 v[200:203], a[28:31], v[172:175], v[200:203]\n\t"
    "ds_read_b64_tr_b16 a[66:67], v252 offset:37632\n\t"
    "v_exp_f32_e32 v193, v193\n\t"
    "v_mfma_f32_16x16x32_bf16 v[224:227], v[232:235], v[184:187], v[224:227]\n\t"
    "s_add_u32 s37, s37, 8192\n\t"
    "ds_read_b64_tr_b16 a[96:97], v252 offset:37152\n\t"
    "v_exp_f32_e32 v194, v194\n\t"
    "v_mfma_f32_16x16x32_bf16 v[204:207], a[0:3], v[8:11], 0\n\t"
    "ds_read_b64_tr_b16 a[98:99], v252 offset:37664\n\t"
    "v_exp_f32_e32 v195, v195\n\t"
    "v_mfma_f32_16x16x32_bf16 v[204:207], a[4:7], v[32:35], v[204:207]\n\t"
    "v_cvt_pk_bf16_f32 v190, v192, v193\n\t"
    "v_cvt_pk_bf16_f32 v191, v194, v195\n\t"
    "v_mfma_f32_16x16x32_bf16 v[204:207], a[8:11], v[56:59], v[204:207]\n\t"
    "s_lshl_b32 s47, %5, 9\n\t"
    "ds_read_b64_tr_b16 a[68:69], v252 offset:45568\n\t"
    "v_mfma_f32_16x16x32_bf16 v[204:207], a[12:15], v[80:83], v[204:207]\n\t"
    "s_add_u32 s47, s47, 1024\n\t"
    "ds_write_b64 v253, v[200:201] offset:61184\n\t"
    "v_mbcnt_lo_u32_b32 v242, -1, 0\n\t"
    "v_mbcnt_hi_u32_b32 v242, -1, v242\n\t"
    "v_and_b32_e32 v243, 15, v242\n\t"
    "v_lshl_add_u32 v242, v243, 5, s47\n\t"
    "v_mfma_f32_16x16x32_bf16 v[204:207], a[16:19], v[104:107], v[204:207]\n\t"
    "s_and_b32 s47, %5, 2\n\t"
    "ds_read_b64_tr_b16 a[70:71], v252 offset:46080\n\t"
    "v_mfma_f32_16x16x32_bf16 v[204:207], a[20:23], v[128:131], v[204:207]\n\t"
    "s_lshl_b32 s47, s47, 9\n\t"
    "ds_write_b64 v253, v[202:203] offset:61192\n\t"
    "v_mfma_f32_16x16x32_bf16 v[204:207], a[24:27], v[152:155], v[204:207]\n\t"
    "s_add_u32 s47, s47, 1024\n\t"
    "v_lshl_add_u32 v238, v243, 5, s47\n\t"
    "v_mfma_f32_16x16x32_bf16 v[204:207], a[28:31], v[176:179], v[204:207]\n\t"
    "ds_read_b64_tr_b16 a[100:101], v252 offset:45600\n\t"
    "v_mfma_f32_16x16x32_bf16 v[228:231], v[232:235], v[188:191], v[228:231]\n\t"
    "ds_read_b64_tr_b16 a[102:103], v252 offset:46112\n\t"
    "v_add_u32_e32 v252, 0xc600, v252\n\t"
    "v_mfma_f32_16x16x32_bf16 v[196:199], a[32:35], v[0:3], 0\n\t"
    "ds_read_b64_tr_b16 a[72:73], v252 offset:3328\n\t"
    "v_mfma_f32_16x16x32_bf16 v[196:199], a[36:39], v[24:27], v[196:199]\n\t"
    "ds_read_b64_tr_b16 a[74:75], v252 offset:3840\n\t"
    "v_mfma_f32_16x16x32_bf16 v[196:199], a[40:43], v[48:51], v[196:199]\n\t"
    "buffer_load_dword v241, v237, %3, s37 offen \n\t"
    "v_mfma_f32_16x16x32_bf16 v[196:199], a[44:47], v[72:75], v[196:199]\n\t"
    "s_waitcnt lgkmcnt(7)\n\t"
    "ds_write_b64 v253, v[204:205] offset:63232\n\t"
    "v_mfma_f32_16x16x32_bf16 v[196:199], a[48:51], v[96:99], v[196:199]\n\t"
    "v_mov_b32_e32 v209, v212\n\t"
    "v_mov_b32_e32 v210, v216\n\t"
    "v_mfma_f32_16x16x32_bf16 v[196:199], a[52:55], v[120:123], v[196:199]\n\t"
    "ds_write_b64 v253, v[206:207] offset:63240\n\t"
    "v_mfma_f32_16x16x32_bf16 v[196:199], a[56:59], v[144:147], v[196:199]\n\t"
    "v_mov_b32_e32 v211, v220\n\t"
    "v_mov_b32_e32 v225, v228\n\t"
    "v_mfma_f32_16x16x32_bf16 v[196:199], a[60:63], v[168:171], v[196:199]\n\t"
    "ds_read_b64_tr_b16 a[104:105], v252 offset:3360\n\t"
    "v_mfma_f32_16x16x32_bf16 v[192:195], a[32:35], v[4:7], 0\n\t"
    "ds_read_b64_tr_b16 a[106:107], v252 offset:3872\n\t"
    "v_mfma_f32_16x16x32_bf16 v[192:195], a[36:39], v[28:31], v[192:195]\n\t"
    "ds_read_b64_tr_b16 a[76:77], v252 offset:11776\n\t"
    "v_mfma_f32_16x16x32_bf16 v[192:195], a[40:43], v[52:55], v[192:195]\n\t"
    "ds_write_b128 v242, v[208:211] offset:0\n\t"
    "v_mfma_f32_16x16x32_bf16 v[192:195], a[44:47], v[76:79], v[192:195]\n\t"
    "v_mfma_f32_16x16x32_bf16 v[192:195], a[48:51], v[100:103], v[192:195]\n\t"
    "ds_write_b64 v242, v[224:225] offset:16\n\t"
    "v_mfma_f32_16x16x32_bf16 v[192:195], a[52:55], v[124:127], v[192:195]\n\t"
    "v_mfma_f32_16x16x32_bf16 v[192:195], a[56:59], v[148:151], v[192:195]\n\t"
    "ds_read_b64_tr_b16 a[78:79], v252 offset:12288\n\t"
    "ds_write_b64 v253, v[196:197] offset:60160\n\t"
    "v_mfma_f32_16x16x32_bf16 v[192:195], a[60:63], v[172:175], v[192:195]\n\t"
    "s_add_u32 s37, s37, 8192\n\t"
    "v_mfma_f32_16x16x32_bf16 v[200:203], a[32:35], v[8:11], 0\n\t"
    "ds_read_b64_tr_b16 a[108:109], v252 offset:11808\n\t"
    "ds_write_b64 v253, v[198:199] offset:60168\n\t"
    "v_mfma_f32_16x16x32_bf16 v[200:203], a[36:39], v[32:35], v[200:203]\n\t"
    "v_mfma_f32_16x16x32_bf16 v[200:203], a[40:43], v[56:59], v[200:203]\n\t"
    "ds_read_b64_tr_b16 a[110:111], v252 offset:12320\n\t"
    "v_mfma_f32_16x16x32_bf16 v[200:203], a[44:47], v[80:83], v[200:203]\n\t"
    "v_mfma_f32_16x16x32_bf16 v[200:203], a[48:51], v[104:107], v[200:203]\n\t"
    "ds_write_b64 v253, v[192:193] offset:62208\n\t"
    "v_mfma_f32_16x16x32_bf16 v[200:203], a[52:55], v[128:131], v[200:203]\n\t"
    "v_mfma_f32_16x16x32_bf16 v[200:203], a[56:59], v[152:155], v[200:203]\n\t"
    "ds_write_b64 v253, v[194:195] offset:62216\n\t"
    "v_mfma_f32_16x16x32_bf16 v[200:203], a[60:63], v[176:179], v[200:203]\n\t"
    "s_waitcnt lgkmcnt(2)\n\t"
    "s_barrier\n\t"
    "ds_read_b128 v[212:215], v238 offset:0\n\t"
    "v_mfma_f32_16x16x32_bf16 v[216:219], a[0:3], v[12:15], 0\n\t"
    "ds_read_b128 v[204:207], v238 offset:512\n\t"
    "v_add_u32_e32 v252, 0xffff3a00, v252\n\t"
    "v_mfma_f32_16x16x32_bf16 v[216:219], a[4:7], v[36:39], v[216:219]\n\t"
    "ds_read_b64_tr_b16 a[80:81], v252 offset:20224\n\t"
    "v_mfma_f32_16x16x32_bf16 v[216:219], a[8:11], v[60:63], v[216:219]\n\t"
    "ds_read_b64 v[220:221], v238 offset:16\n\t"
    "v_mfma_f32_16x16x32_bf16 v[216:219], a[12:15], v[84:87], v[216:219]\n\t"
    "ds_write_b64 v253, v[200:201] offset:64256\n\t"
    "v_mfma_f32_16x16x32_bf16 v[216:219], a[16:19], v[108:111], v[216:219]\n\t"
    "v_mfma_f32_16x16x32_bf16 v[216:219], a[20:23], v[132:135], v[216:219]\n\t"
    "ds_write_b64 v253, v[202:203] offset:64264\n\t"
    "v_mfma_f32_16x16x32_bf16 v[216:219], a[24:27], v[156:159], v[216:219]\n\t"
    "v_mfma_f32_16x16x32_bf16 v[216:219], a[28:31], v[180:183], v[216:219]\n\t"
    "ds_read_b64_tr_b16 a[82:83], v252 offset:20736\n\t"
    "v_mfma_f32_16x16x32_bf16 v[228:231], a[32:35], v[12:15], 0\n\t"
    "ds_read_b64 v[222:223], v238 offset:528\n\t"
    "v_mfma_f32_16x16x32_bf16 v[228:231], a[36:39], v[36:39], v[228:231]\n\t"
    "ds_read_b64_tr_b16 a[112:113], v252 offset:20256\n\t"
    "v_mfma_f32_16x16x32_bf16 v[228:231], a[40:43], v[60:63], v[228:231]\n\t"
    "ds_read_b64_tr_b16 a[114:115], v252 offset:20768\n\t"
    "v_mfma_f32_16x16x32_bf16 v[228:231], a[44:47], v[84:87], v[228:231]\n\t"
    "ds_read_b64_tr_b16 a[84:85], v252 offset:28672\n\t"
    "v_mfma_f32_16x16x32_bf16 v[228:231], a[48:51], v[108:111], v[228:231]\n\t"
    "ds_write_b64 v253, v[216:217] offset:0\n\t"
    "v_mfma_f32_16x16x32_bf16 v[228:231], a[52:55], v[132:135], v[228:231]\n\t"
    "v_mfma_f32_16x16x32_bf16 v[228:231], a[56:59], v[156:159], v[228:231]\n\t"
    "ds_read_b64_tr_b16 a[86:87], v252 offset:29184\n\t"
    "v_mfma_f32_16x16x32_bf16 v[228:231], a[60:63], v[180:183], v[228:231]\n\t"
    "ds_write_b64 v253, v[218:219] offset:8\n\t"
    "v_mfma_f32_16x16x32_bf16 v[208:211], a[0:3], v[16:19], 0\n\t"
    "s_waitcnt lgkmcnt(9)\n\t"
    "v_add_f32_e32 v232, v212, v204\n\t"
    "v_add_f32_e32 v233, v213, v205\n\t"
    "v_mfma_f32_16x16x32_bf16 v[208:211], a[4:7], v[40:43], v[208:211]\n\t"
    "ds_read_b64_tr_b16 a[116:117], v252 offset:28704\n\t"
    "v_add_f32_e32 v234, v214, v206\n\t"
    "v_mfma_f32_16x16x32_bf16 v[208:211], a[8:11], v[64:67], v[208:211]\n\t"
    "ds_read_b64_tr_b16 a[118:119], v252 offset:29216\n\t"
    "v_add_u32_e32 v252, 0x25200, v252\n\t"
    "v_mfma_f32_16x16x32_bf16 v[208:211], a[12:15], v[88:91], v[208:211]\n\t"
    "s_waitcnt lgkmcnt(8)\n\t"
    "v_add_f32_e32 v235, v215, v207\n\t"
    "v_add_f32_e32 v236, v220, v222\n\t"
    "v_mfma_f32_16x16x32_bf16 v[208:211], a[16:19], v[112:115], v[208:211]\n\t"
    "ds_write_b64 v253, v[228:229] offset:1024\n\t"
    "v_mfma_f32_16x16x32_bf16 v[208:211], a[20:23], v[136:139], v[208:211]\n\t"
    "v_add_f32_e32 v237, v221, v223\n\t"
    "v_mfma_f32_16x16x32_bf16 v[208:211], a[24:27], v[160:163], v[208:211]\n\t"
    "ds_read_b64_tr_b16 a[88:89], v252 offset:3328\n\t"
    "v_mfma_f32_16x16x32_bf16 v[208:211], a[28:31], v[184:187], v[208:211]\n\t"
    "ds_write_b64 v253, v[230:231] offset:1032\n\t"
    "v_mfma_f32_16x16x32_bf16 v[224:227], a[32:35], v[16:19], 0\n\t"
    "v_rcp_f32_e32 v244, v232\n\t"
    "v_mfma_f32_16x16x32_bf16 v[224:227], a[36:39], v[40:43], v[224:227]\n\t"
    "ds_read_b64_tr_b16 a[90:91], v252 offset:3840\n\t"
    "v_rcp_f32_e32 v245, v233\n\t"
    "v_mfma_f32_16x16x32_bf16 v[224:227], a[40:43], v[64:67], v[224:227]\n\t"
    "ds_read_b64_tr_b16 a[120:121], v252 offset:3360\n\t"
    "v_rcp_f32_e32 v246, v234\n\t"
    "v_mfma_f32_16x16x32_bf16 v[224:227], a[44:47], v[88:91], v[224:227]\n\t"
    "v_rcp_f32_e32 v247, v235\n\t"
    "v_mfma_f32_16x16x32_bf16 v[224:227], a[48:51], v[112:115], v[224:227]\n\t"
    "ds_read_b64_tr_b16 a[122:123], v252 offset:3872\n\t"
    "v_rcp_f32_e32 v248, v236\n\t"
    "v_mfma_f32_16x16x32_bf16 v[224:227], a[52:55], v[136:139], v[224:227]\n\t"
    "ds_write_b64 v253, v[208:209] offset:2048\n\t"
    "v_mfma_f32_16x16x32_bf16 v[224:227], a[56:59], v[160:163], v[224:227]\n\t"
    "s_add_u32 m0, s42, 36864\n\t"
    "v_rcp_f32_e32 v249, v237\n\t"
    "v_add_u32_e32 v252, 0xfffdae00, v252\n\t"
    "v_mfma_f32_16x16x32_bf16 v[224:227], a[60:63], v[184:187], v[224:227]\n\t"
    "ds_read_b128 v[196:199], v254 offset:59136\n\t"
    "v_add_f32_e32 v232, v232, v233\n\t"
    "v_mfma_f32_16x16x32_bf16 v[192:195], a[0:3], v[20:23], 0\n\t"
    "ds_write_b64 v253, v[210:211] offset:2056\n\t"
    "v_mfma_f32_16x16x32_bf16 v[192:195], a[4:7], v[44:47], v[192:195]\n\t"
    "v_add_f32_e32 v234, v234, v235\n\t"
    "v_add_f32_e32 v236, v236, v237\n\t"
    "v_mfma_f32_16x16x32_bf16 v[192:195], a[8:11], v[68:71], v[192:195]\n\t"
    "ds_read_b64_tr_b16 a[92:93], v252 offset:3328\n\t"
    "v_add_f32_e32 v232, v232, v234\n\t"
    "v_mfma_f32_16x16x32_bf16 v[192:195], a[12:15], v[92:95], v[192:195]\n\t"
    "ds_read_b64_tr_b16 a[94:95], v252 offset:3840\n\t"
    "v_add_f32_e32 v232, v232, v236\n\t"
    "v_mfma_f32_16x16x32_bf16 v[192:195], a[16:19], v[116:119], v[192:195]\n\t"
    "buffer_load_dwordx4 v250, %3, s53 offen offset:256  lds\n\t"
    "v_cmp_nge_f32_e32 vcc, s44, v232\n\t"
    "v_mfma_f32_16x16x32_bf16 v[192:195], a[20:23], v[140:143], v[192:195]\n\t"
    "s_nop 1\n\t"
    "ds_read_b64_tr_b16 a[124:125], v252 offset:3360\n\t"
    "v_mfma_f32_16x16x32_bf16 v[192:195], a[24:27], v[164:167], v[192:195]\n\t"
    "ds_write_b64 v253, v[224:225] offset:3072\n\t"
    "v_mfma_f32_16x16x32_bf16 v[192:195], a[28:31], v[188:191], v[192:195]\n\t"
    "s_or_b32 s43, vcc_lo, vcc_hi\n\t"
    "v_mfma_f32_16x16x32_bf16 v[200:203], a[32:35], v[20:23], 0\n\t"
    "s_lshl_b32 s47, %5, 2\n\t"
    "ds_read_b128 v[216:219], v254 offset:60160\n\t"
    "ds_read_b64_tr_b16 a[126:127], v252 offset:3872\n\t"
    "v_mfma_f32_16x16x32_bf16 v[200:203], a[36:39], v[44:47], v[200:203]\n\t"
    "s_add_u32 m0, s42, 37408\n\t"
    "ds_write_b64 v253, v[226:227] offset:3080\n\t"
    "v_mfma_f32_16x16x32_bf16 v[200:203], a[40:43], v[68:71], v[200:203]\n\t"
    "s_add_u32 s47, s47, 3072\n\t"
    "v_mov_b32_e32 v242, s47\n\t"
    "v_mov_b32_e32 v243, s43\n\t"
    "v_mfma_f32_16x16x32_bf16 v[200:203], a[44:47], v[92:95], v[200:203]\n\t"
    "ds_write_b32 v242, v243 offset:0\n\t"
    "v_mfma_f32_16x16x32_bf16 v[200:203], a[48:51], v[116:119], v[200:203]\n\t"
    "buffer_load_dwordx4 v250, %3, s53 offen offset:768  lds\n\t"
    "v_mfma_f32_16x16x32_bf16 v[200:203], a[52:55], v[140:143], v[200:203]\n\t"
    "ds_write_b64 v253, v[192:193] offset:4096\n\t"
    "v_mfma_f32_16x16x32_bf16 v[200:203], a[56:59], v[164:167], v[200:203]\n\t"
    "ds_write_b64 v253, v[194:195] offset:4104\n\t"
    "v_mfma_f32_16x16x32_bf16 v[200:203], a[60:63], v[188:191], v[200:203]\n\t"
    "s_waitcnt vmcnt(14) lgkmcnt(4)\n\t"
    "s_barrier\n\t"
    "ds_read_b128 v[204:207], v254 offset:61184\n\t"
    "v_mfma_f32_16x16x32_bf16 v[196:199], a[64:67], v[0:3], v[196:199]\n\t"
    "s_add_u32 m0, s42, 19968\n\t"
    "ds_read_b64_tr_b16 a[128:129], v251 offset:11776\n\t"
    "v_mfma_f32_16x16x32_bf16 v[196:199], a[68:71], v[24:27], v[196:199]\n\t"
    "ds_read_b64_tr_b16 a[130:131], v251 offset:12288\n\t"
    "v_mfma_f32_16x16x32_bf16 v[196:199], a[72:75], v[48:51], v[196:199]\n\t"
    "ds_read_b64_tr_b16 a[160:161], v251 offset:11808\n\t"
    "v_mfma_f32_16x16x32_bf16 v[196:199], a[76:79], v[72:75], v[196:199]\n\t"
    "ds_write_b64 v253, v[200:201] offset:5120\n\t"
    "v_mfma_f32_16x16x32_bf16 v[196:199], a[80:83], v[96:99], v[196:199]\n\t"
    "buffer_load_dwordx4 v250, %3, s55 offen offset:256  lds\n\t"
    "v_mfma_f32_16x16x32_bf16 v[196:199], a[84:87], v[120:123], v[196:199]\n\t"
    "ds_read_b128 v[212:215], v254 offset:62208\n\t"
    "v_mfma_f32_16x16x32_bf16 v[196:199], a[88:91], v[144:147], v[196:199]\n\t"
    "ds_write_b64 v253, v[202:203] offset:5128\n\t"
    "v_mfma_f32_16x16x32_bf16 v[196:199], a[92:95], v[168:171], v[196:199]\n\t"
    "s_add_u32 m0, s42, 20512\n\t"
    "v_mfma_f32_16x16x32_bf16 v[216:219], a[96:99], v[0:3], v[216:219]\n\t"
    "ds_read_b64_tr_b16 a[162:163], v251 offset:12320\n\t"
    "v_mfma_f32_16x16x32_bf16 v[216:219], a[100:103], v[24:27], v[216:219]\n\t"
    "ds_read_b64_tr_b16 a[192:193], v252 offset:11776\n\t"
    "v_mfma_f32_16x16x32_bf16 v[216:219], a[104:107], v[48:51], v[216:219]\n\t"
    "ds_read_b64_tr_b16 a[194:195], v252 offset:12288\n\t"
    "v_mfma_f32_16x16x32_bf16 v[216:219], a[108:111], v[72:75], v[216:219]\n\t"
    "ds_read_b64_tr_b16 a[224:225], v252 offset:11808\n\t"
    "v_mfma_f32_16x16x32_bf16 v[216:219], a[112:115], v[96:99], v[216:219]\n\t"
    "buffer_load_dwordx4 v250, %3, s55 offen offset:768  lds\n\t"
    "v_mul_f32_e32 v196, v196, v244\n\t"
    "v_mfma_f32_16x16x32_bf16 v[216:219], a[116:119], v[120:123], v[216:219]\n\t"
    "s_add_u32 m0, s42, 28416\n\t"
    "ds_read_b64_tr_b16 a[226:227], v252 offset:12320\n\t"
    "v_add_u32_e32 v251, 0xc600, v251\n\t"
    "v_mul_f32_e32 v197, v197, v244\n\t"
    "v_mfma_f32_16x16x32_bf16 v[216:219], a[120:123], v[144:147], v[216:219]\n\t"
    "ds_read_b128 v[220:223], v254 offset:63232\n\t"
    "v_mul_f32_e32 v198, v198, v244\n\t"
    "v_mfma_f32_16x16x32_bf16 v[216:219], a[124:127], v[168:171], v[216:219]\n\t"
    "s_waitcnt lgkmcnt(8)\n\t"
    "ds_read_b64_tr_b16 a[132:133], v251 offset:20224\n\t"
    "v_mul_f32_e32 v199, v199, v244\n\t"
    "v_mfma_f32_16x16x32_bf16 v[204:207], a[64:67], v[4:7], v[204:207]\n\t"
    "ds_read_b64_tr_b16 a[134:135], v251 offset:20736\n\t"
    "v_cvt_pk_bf16_f32 v196, v196, v197\n\t"
    "v_mfma_f32_16x16x32_bf16 v[204:207], a[68:71], v[28:31], v[204:207]\n\t"
    "buffer_load_dwordx4 v250, %3, s57 offen offset:256  lds\n\t"
    "v_cvt_pk_bf16_f32 v197, v198, v199\n\t"
    "v_mfma_f32_16x16x32_bf16 v[204:207], a[72:75], v[52:55], v[204:207]\n\t"
    "s_add_u32 m0, s42, 28960\n\t"
    "ds_read_b64_tr_b16 a[164:165], v251 offset:20256\n\t"
    "v_mfma_f32_16x16x32_bf16 v[204:207], a[76:79], v[76:79], v[204:207]\n\t"
    "ds_read_b64_tr_b16 a[166:167], v251 offset:20768\n\t"
    "v_add_u32_e32 v252, 0xc600, v252\n\t"
    "v_mfma_f32_16x16x32_bf16 v[204:207], a[80:83], v[100:103], v[204:207]\n\t"
    "ds_read_b128 v[228:231], v254 offset:64256\n\t"
    "v_mul_f32_e32 v216, v216, v244\n\t"
    "v_mfma_f32_16x16x32_bf16 v[204:207], a[84:87], v[124:127], v[204:207]\n\t"
    "ds_read_b64_tr_b16 a[196:197], v252 offset:20224\n\t"
    "v_mul_f32_e32 v217, v217, v244\n\t"
    "v_mfma_f32_16x16x32_bf16 v[204:207], a[88:91], v[148:151], v[204:207]\n\t"
    "buffer_load_dwordx4 v250, %3, s57 offen offset:768  lds\n\t"
    "v_mul_f32_e32 v218, v218, v244\n\t"
    "v_mfma_f32_16x16x32_bf16 v[204:207], a[92:95], v[172:175], v[204:207]\n\t"
    "s_add_u32 m0, s42, 155136\n\t"
    "ds_read_b64_tr_b16 a[198:199], v252 offset:20736\n\t"
    "v_mul_f32_e32 v219, v219, v244\n\t"
    "s_waitcnt lgkmcnt(9)\n\t"
    "v_mfma_f32_16x16x32_bf16 v[212:215], a[96:99], v[4:7], v[212:215]\n\t"
    "ds_read_b64_tr_b16 a[228:229], v252 offset:20256\n\t"
    "v_cvt_pk_bf16_f32 v198, v216, v217\n\t"
    "v_mfma_f32_16x16x32_bf16 v[212:215], a[100:103], v[28:31], v[212:215]\n\t"
    "ds_read_b64_tr_b16 a[230:231], v252 offset:20768\n\t"
    "v_cvt_pk_bf16_f32 v199, v218, v219\n\t"
    "v_mfma_f32_16x16x32_bf16 v[212:215], a[104:107], v[52:55], v[212:215]\n\t"
    "v_permlane16_swap_b32_e32 v196, v198\n\t"
    "v_mfma_f32_16x16x32_bf16 v[212:215], a[108:111], v[76:79], v[212:215]\n\t"
    "v_permlane16_swap_b32_e32 v197, v199\n\t"
    "v_mfma_f32_16x16x32_bf16 v[212:215], a[112:115], v[100:103], v[212:215]\n\t"
    "ds_read_b64_tr_b16 a[136:137], v251 offset:28672\n\t"
    "v_mul_f32_e32 v204, v204, v245\n\t"
    "v_mfma_f32_16x16x32_bf16 v[212:215], a[116:119], v[124:127], v[212:215]\n\t"
    "buffer_load_dwordx4 v250, %3, s59 offen offset:256  lds\n\t"
    "v_mul_f32_e32 v205, v205, v245\n\t"
    "v_mfma_f32_16x16x32_bf16 v[212:215], a[120:123], v[148:151], v[212:215]\n\t"
    "s_add_u32 m0, s42, 155680\n\t"
    "ds_read_b128 v[208:211], v254 offset:0\n\t"
    "v_mul_f32_e32 v206, v206, v245\n\t"
    "v_mfma_f32_16x16x32_bf16 v[212:215], a[124:127], v[172:175], v[212:215]\n\t"
    "s_waitcnt lgkmcnt(6)\n\t"
    "ds_read_b64_tr_b16 a[138:139], v251 offset:29184\n\t"
    "v_mul_f32_e32 v207, v207, v245\n\t"
    "v_mfma_f32_16x16x32_bf16 v[220:223], a[64:67], v[8:11], v[220:223]\n\t"
    "ds_read_b64_tr_b16 a[168:169], v251 offset:28704\n\t"
    "v_cvt_pk_bf16_f32 v204, v204, v205\n\t"
    "v_mfma_f32_16x16x32_bf16 v[220:223], a[68:71], v[32:35], v[220:223]\n\t"
    "ds_read_b64_tr_b16 a[170:171], v251 offset:29216\n\t"
    "v_cvt_pk_bf16_f32 v205, v206, v207\n\t"
    "v_mfma_f32_16x16x32_bf16 v[220:223], a[72:75], v[56:59], v[220:223]\n\t"
    "ds_read_b64_tr_b16 a[200:201], v252 offset:28672\n\t"
    "v_mfma_f32_16x16x32_bf16 v[220:223], a[76:79], v[80:83], v[220:223]\n\t"
    "buffer_load_dwordx4 v250, %3, s59 offen offset:768  lds\n\t"
    "v_mfma_f32_16x16x32_bf16 v[220:223], a[80:83], v[104:107], v[220:223]\n\t"
    "s_add_u32 m0, s42, 3072\n\t"
    "ds_read_b64_tr_b16 a[202:203], v252 offset:29184\n\t"
    "v_mul_f32_e32 v212, v212, v245\n\t"
    "v_mfma_f32_16x16x32_bf16 v[220:223], a[84:87], v[128:131], v[220:223]\n\t"
    "ds_read_b64_tr_b16 a[232:233], v252 offset:28704\n\t"
    "v_mul_f32_e32 v213, v213, v245\n\t"
    "v_mfma_f32_16x16x32_bf16 v[220:223], a[88:91], v[152:155], v[220:223]\n\t"
    "ds_read_b128 v[236:239], v254 offset:1024\n\t"
    "v_mul_f32_e32 v214, v214, v245\n\t"
    "v_mfma_f32_16x16x32_bf16 v[220:223], a[92:95], v[176:179], v[220:223]\n\t"
    "ds_read_b64_tr_b16 a[234:235], v252 offset:29216\n\t"
    "v_mul_f32_e32 v215, v215, v245\n\t"
    "v_mfma_f32_16x16x32_bf16 v[228:231], a[96:99], v[8:11], v[228:231]\n\t"
    "buffer_load_dwordx4 v250, %3, s61 offen offset:256  lds\n\t"
    "v_cvt_pk_bf16_f32 v206, v212, v213\n\t"
    "v_mfma_f32_16x16x32_bf16 v[228:231], a[100:103], v[32:35], v[228:231]\n\t"
    "s_add_u32 m0, s42, 3616\n\t"
    "v_cvt_pk_bf16_f32 v207, v214, v215\n\t"
    "v_mfma_f32_16x16x32_bf16 v[228:231], a[104:107], v[56:59], v[228:231]\n\t"
    "v_permlane16_swap_b32_e32 v204, v206\n\t"
    "v_mfma_f32_16x16x32_bf16 v[228:231], a[108:111], v[80:83], v[228:231]\n\t"
    "v_permlane16_swap_b32_e32 v205, v207\n\t"
    "v_mfma_f32_16x16x32_bf16 v[228:231], a[112:115], v[104:107], v[228:231]\n\t"
    "ds_read_b128 v[232:235], v254 offset:2048\n\t"
    "v_mul_f32_e32 v220, v220, v246\n\t"
    "v_mfma_f32_16x16x32_bf16 v[228:231], a[116:119], v[128:131], v[228:231]\n\t"
    "buffer_load_dwordx4 v250, %3, s61 offen offset:768  lds\n\t"
    "v_mul_f32_e32 v221, v221, v246\n\t"
    "v_mfma_f32_16x16x32_bf16 v[228:231], a[120:123], v[152:155], v[228:231]\n\t"
    "v_mul_f32_e32 v222, v222, v246\n\t"
    "v_mul_f32_e32 v223, v223, v246\n\t"
    "v_mfma_f32_16x16x32_bf16 v[228:231], a[124:127], v[176:179], v[228:231]\n\t"
    "s_waitcnt vmcnt(12) lgkmcnt(1)\n\t"
    "s_barrier\n\t"
    "v_cvt_pk_bf16_f32 v220, v220, v221\n\t"
    "v_mfma_f32_16x16x32_bf16 v[208:211], a[64:67], v[12:15], v[208:211]\n\t"
    "s_add_u32 m0, s42, 11520\n\t"
    "ds_read_b64_tr_b16 a[140:141], v251 offset:37120\n\t"
    "v_cvt_pk_bf16_f32 v221, v222, v223\n\t"
    "v_mfma_f32_16x16x32_bf16 v[208:211], a[68:71], v[36:39], v[208:211]\n\t"
    "ds_read_b64_tr_b16 a[142:143], v251 offset:37632\n\t"
    "v_mov_b32_e32 v242, 3072\n\t"
    "v_mfma_f32_16x16x32_bf16 v[208:211], a[72:75], v[60:63], v[208:211]\n\t"
    "ds_read_b64_tr_b16 a[172:173], v251 offset:37152\n\t"
    "v_mfma_f32_16x16x32_bf16 v[208:211], a[76:79], v[84:87], v[208:211]\n\t"
    "ds_read_b128 v[224:227], v254 offset:3072\n\t"
    "v_mul_f32_e32 v228, v228, v246\n\t"
    "v_mfma_f32_16x16x32_bf16 v[208:211], a[80:83], v[108:111], v[208:211]\n\t"
    "ds_read_b128 v[192:195], v242 offset:0\n\t"
    "v_mul_f32_e32 v229, v229, v246\n\t"
    "v_mfma_f32_16x16x32_bf16 v[208:211], a[84:87], v[132:135], v[208:211]\n\t"
    "ds_read_b64_tr_b16 a[174:175], v251 offset:37664\n\t"
    "v_mul_f32_e32 v230, v230, v246\n\t"
    "v_mfma_f32_16x16x32_bf16 v[208:211], a[88:91], v[156:159], v[208:211]\n\t"
    "buffer_load_dwordx4 v250, %3, s63 offen offset:256  lds\n\t"
    "v_mul_f32_e32 v231, v231, v246\n\t"
    "v_mfma_f32_16x16x32_bf16 v[208:211], a[92:95], v[180:183], v[208:211]\n\t"
    "ds_read_b64_tr_b16 a[144:145], v251 offset:45568\n\t"
    "v_cvt_pk_bf16_f32 v222, v228, v229\n\t"
    "v_mfma_f32_16x16x32_bf16 v[236:239], a[96:99], v[12:15], v[236:239]\n\t"
    "s_add_u32 m0, s42, 12064\n\t"
    "ds_read_b64_tr_b16 a[146:147], v251 offset:46080\n\t"
    "v_cvt_pk_bf16_f32 v223, v230, v231\n\t"
    "v_mfma_f32_16x16x32_bf16 v[236:239], a[100:103], v[36:39], v[236:239]\n\t"
    "v_permlane16_swap_b32_e32 v220, v222\n\t"
    "v_mfma_f32_16x16x32_bf16 v[236:239], a[104:107], v[60:63], v[236:239]\n\t"
    "ds_read_b64_tr_b16 a[176:177], v251 offset:45600\n\t"
    "v_permlane16_swap_b32_e32 v221, v223\n\t"
    "v_mfma_f32_16x16x32_bf16 v[236:239], a[108:111], v[84:87], v[236:239]\n\t"
    "ds_read_b64_tr_b16 a[178:179], v251 offset:46112\n\t"
    "v_add_u32_e32 v251, 0xc600, v251\n\t"
    "v_mfma_f32_16x16x32_bf16 v[236:239], a[112:115], v[108:111], v[236:239]\n\t"
    "ds_read_b128 v[200:203], v254 offset:4096\n\t"
    "v_mul_f32_e32 v208, v208, v247\n\t"
    "v_mfma_f32_16x16x32_bf16 v[236:239], a[116:119], v[132:135], v[236:239]\n\t"
    "buffer_load_dwordx4 v250, %3, s63 offen offset:768  lds\n\t"
    "v_mul_f32_e32 v209, v209, v247\n\t"
    "v_mfma_f32_16x16x32_bf16 v[236:239], a[120:123], v[156:159], v[236:239]\n\t"
    "ds_read_b64_tr_b16 a[148:149], v251 offset:37120\n\t"
    "v_mul_f32_e32 v210, v210, v247\n\t"
    "v_mfma_f32_16x16x32_bf16 v[236:239], a[124:127], v[180:183], v[236:239]\n\t"
    "s_waitcnt lgkmcnt(7)\n\t"
    "v_mul_f32_e32 v211, v211, v247\n\t"
    "v_cvt_pk_bf16_f32 v208, v208, v209\n\t"
    "v_mfma_f32_16x16x32_bf16 v[232:235], a[64:67], v[16:19], v[232:235]\n\t"
    "s_add_u32 m0, s42, 70656\n\t"
    "ds_read_b64_tr_b16 a[150:151], v251 offset:37632\n\t"
    "v_cvt_pk_bf16_f32 v209, v210, v211\n\t"
    "v_mfma_f32_16x16x32_bf16 v[232:235], a[68:71], v[40:43], v[232:235]\n\t"
    "ds_read_b64_tr_b16 a[180:181], v251 offset:37152\n\t"
    "v_mfma_f32_16x16x32_bf16 v[232:235], a[72:75], v[64:67], v[232:235]\n\t"
    "ds_read_b64_tr_b16 a[182:183], v251 offset:37664\n\t"
    "v_mfma_f32_16x16x32_bf16 v[232:235], a[76:79], v[88:91], v[232:235]\n\t"
    "ds_read_b128 v[216:219], v254 offset:5120\n\t"
    "v_mfma_f32_16x16x32_bf16 v[232:235], a[80:83], v[112:115], v[232:235]\n\t"
    "ds_read_b64_tr_b16 a[152:153], v251 offset:45568\n\t"
    "v_mul_f32_e32 v236, v236, v247\n\t"
    "v_mfma_f32_16x16x32_bf16 v[232:235], a[84:87], v[136:139], v[232:235]\n\t"
    "v_or_b32_e32 v192, v192, v193\n\t"
    "v_mul_f32_e32 v237, v237, v247\n\t"
    "v_mfma_f32_16x16x32_bf16 v[232:235], a[88:91], v[160:163], v[232:235]\n\t"
    "buffer_load_dwordx4 v250, %3, s65 offen offset:256  lds\n\t"
    "v_mul_f32_e32 v238, v238, v247\n\t"
    "v_mfma_f32_16x16x32_bf16 v[232:235], a[92:95], v[184:187], v[232:235]\n\t"
    "ds_read_b64_tr_b16 a[154:155], v251 offset:46080\n\t"
    "v_mul_f32_e32 v239, v239, v247\n\t"
    "v_mfma_f32_16x16x32_bf16 v[224:227], a[96:99], v[16:19], v[224:227]\n\t"
    "ds_read_b64_tr_b16 a[184:185], v251 offset:45600\n\t"
    "v_or3_b32 v243, v192, v194, v195\n\t"
    "v_cvt_pk_bf16_f32 v210, v236, v237\n\t"
    "v_mfma_f32_16x16x32_bf16 v[224:227], a[100:103], v[40:43], v[224:227]\n\t"
    "s_add_u32 m0, s42, 71200\n\t"
    "ds_read_b64_tr_b16 a[186:187], v251 offset:46112\n\t"
    "v_cvt_pk_bf16_f32 v211, v238, v239\n\t"
    "v_mfma_f32_16x16x32_bf16 v[224:227], a[104:107], v[64:67], v[224:227]\n\t"
    "s_nop 0\n\t"
    "v_permlane16_swap_b32_e32 v208, v210\n\t"
    "v_mfma_f32_16x16x32_bf16 v[224:227], a[108:111], v[88:91], v[224:227]\n\t"
    "ds_read_b64_tr_b16 a[156:157], v251 offset:28672\n\t"
    "v_permlane16_swap_b32_e32 v209, v211\n\t"
    "v_mfma_f32_16x16x32_bf16 v[224:227], a[112:115], v[112:115], v[224:227]\n\t"
    "PG5_%=:\n\t"
    "s_cbranch_execz RET_%=\n\t"
    "v_mul_f32_e32 v232, v232, v248\n\t"
    "v_readfirstlane_b32 %0, v243\n\t"
    "v_mfma_f32_16x16x32_bf16 v[224:227], a[116:119], v[136:139], v[224:227]\n\t"
    "ds_read_b64_tr_b16 a[158:159], v251 offset:29184\n\t"
    "v_mul_f32_e32 v233, v233, v248\n\t"
    "v_mfma_f32_16x16x32_bf16 v[224:227], a[120:123], v[160:163], v[224:227]\n\t"
    "buffer_load_dwordx4 v250, %3, s65 offen offset:768  lds\n\t"
    "v_mul_f32_e32 v234, v234, v248\n\t"
    "v_mfma_f32_16x16x32_bf16 v[224:227], a[124:127], v[184:187], v[224:227]\n\t"
    "s_waitcnt lgkmcnt(5)\n\t"
    "ds_read_b64_tr_b16 a[188:189], v251 offset:28704\n\t"
    "v_mul_f32_e32 v235, v235, v248\n\t"
    "v_mfma_f32_16x16x32_bf16 v[200:203], a[64:67], v[20:23], v[200:203]\n\t"
    "s_add_u32 m0, s42, 79104\n\t"
    "ds_read_b64_tr_b16 a[190:191], v251 offset:29216\n\t"
    "v_cvt_pk_bf16_f32 v232, v232, v233\n\t"
    "v_mfma_f32_16x16x32_bf16 v[200:203], a[68:71], v[44:47], v[200:203]\n\t"
    "ds_read_b64_tr_b16 a[204:205], v252 offset:37120\n\t"
    "v_cvt_pk_bf16_f32 v233, v234, v235\n\t"
    "v_mfma_f32_16x16x32_bf16 v[200:203], a[72:75], v[68:71], v[200:203]\n\t"
    "ds_read_b64_tr_b16 a[206:207], v252 offset:37632\n\t"
    "v_mfma_f32_16x16x32_bf16 v[200:203], a[76:79], v[92:95], v[200:203]\n\t"
    "ds_read_b64_tr_b16 a[236:237], v252 offset:37152\n\t"
    "v_mfma_f32_16x16x32_bf16 v[200:203], a[80:83], v[116:119], v[200:203]\n\t"
    "buffer_load_dwordx4 v250, %3, s67 offen offset:256  lds\n\t"
    "v_mul_f32_e32 v224, v224, v248\n\t"
    "v_mfma_f32_16x16x32_bf16 v[200:203], a[84:87], v[140:143], v[200:203]\n\t"
    "ds_read_b64_tr_b16 a[238:239], v252 offset:37664\n\t"
    "v_mul_f32_e32 v225, v225, v248\n\t"
    "v_mfma_f32_16x16x32_bf16 v[200:203], a[88:91], v[164:167], v[200:203]\n\t"
    "v_mul_f32_e32 v226, v226, v248\n\t"
    "v_mul_f32_e32 v227, v227, v248\n\t"
    "v_mfma_f32_16x16x32_bf16 v[200:203], a[92:95], v[188:191], v[200:203]\n\t"
    "v_cvt_pk_bf16_f32 v234, v224, v225\n\t"
    "v_cvt_pk_bf16_f32 v235, v226, v227\n\t"
    "v_mfma_f32_16x16x32_bf16 v[216:219], a[96:99], v[20:23], v[216:219]\n\t"
    "s_add_u32 m0, s42, 79648\n\t"
    "v_permlane16_swap_b32_e32 v232, v234\n\t"
    "v_mfma_f32_16x16x32_bf16 v[216:219], a[100:103], v[44:47], v[216:219]\n\t"
    "v_permlane16_swap_b32_e32 v233, v235\n\t"
    "v_mfma_f32_16x16x32_bf16 v[216:219], a[104:107], v[68:71], v[216:219]\n\t"
    "v_mfma_f32_16x16x32_bf16 v[216:219], a[108:111], v[92:95], v[216:219]\n\t"
    "v_mfma_f32_16x16x32_bf16 v[216:219], a[112:115], v[116:119], v[216:219]\n\t"
    "buffer_load_dwordx4 v250, %3, s67 offen offset:768  lds\n\t"
    "v_mul_f32_e32 v200, v200, v249\n\t"
    "v_mfma_f32_16x16x32_bf16 v[216:219], a[116:119], v[140:143], v[216:219]\n\t"
    "v_mul_f32_e32 v201, v201, v249\n\t"
    "v_mul_f32_e32 v202, v202, v249\n\t"
    "v_mfma_f32_16x16x32_bf16 v[216:219], a[120:123], v[164:167], v[216:219]\n\t"
    "v_mul_f32_e32 v203, v203, v249\n\t"
    "v_cvt_pk_bf16_f32 v200, v200, v201\n\t"
    "v_mfma_f32_16x16x32_bf16 v[216:219], a[124:127], v[188:191], v[216:219]\n\t"
    "s_waitcnt vmcnt(14) lgkmcnt(0)\n\t"
    "s_barrier\n\t"
    "v_cvt_pk_bf16_f32 v201, v202, v203\n\t"
    "v_mfma_f32_16x16x32_bf16 v[212:215], a[128:131], v[0:3], 0\n\t"
    "ds_read_b64_tr_b16 a[208:209], v252 offset:45568\n\t"
    "v_mfma_f32_16x16x32_bf16 v[212:215], a[132:135], v[24:27], v[212:215]\n\t"
    "s_add_u32 m0, s42, 87424\n\t"
    "ds_read_b64_tr_b16 a[210:211], v252 offset:46080\n\t"
    "v_mfma_f32_16x16x32_bf16 v[212:215], a[136:139], v[48:51], v[212:215]\n\t"
    "ds_read_b64_tr_b16 a[240:241], v252 offset:45600\n\t"
    "v_mfma_f32_16x16x32_bf16 v[212:215], a[140:143], v[72:75], v[212:215]\n\t"
    "v_mul_f32_e32 v216, v216, v249\n\t"
    "v_mul_f32_e32 v217, v217, v249\n\t"
    "v_mfma_f32_16x16x32_bf16 v[212:215], a[144:147], v[96:99], v[212:215]\n\t"
    "ds_read_b64_tr_b16 a[242:243], v252 offset:46112\n\t"
    "v_mul_f32_e32 v218, v218, v249\n\t"
    "v_mfma_f32_16x16x32_bf16 v[212:215], a[148:151], v[120:123], v[212:215]\n\t"
    "v_mul_f32_e32 v219, v219, v249\n\t"
    "v_cvt_pk_bf16_f32 v202, v216, v217\n\t"
    "v_mfma_f32_16x16x32_bf16 v[212:215], a[152:155], v[144:147], v[212:215]\n\t"
    "buffer_load_dwordx4 v250, %3, s53 offen offset:384  lds\n\t"
    "v_add_u32_e32 v252, 0xc600, v252\n\t"
    "v_mfma_f32_16x16x32_bf16 v[212:215], a[156:159], v[168:171], v[212:215]\n\t"
    "ds_read_b64_tr_b16 a[212:213], v252 offset:37120\n\t"
    "v_cvt_pk_bf16_f32 v203, v218, v219\n\t"
    "v_mfma_f32_16x16x32_bf16 v[228:231], a[160:163], v[0:3], 0\n\t"
    "v_permlane16_swap_b32_e32 v200, v202\n\t"
    "v_mfma_f32_16x16x32_bf16 v[228:231], a[164:167], v[24:27], v[228:231]\n\t"
    "v_permlane16_swap_b32_e32 v201, v203\n\t"
    "v_mfma_f32_16x16x32_bf16 v[228:231], a[168:171], v[48:51], v[228:231]\n\t"
    "ds_read_b64_tr_b16 a[214:215], v252 offset:37632\n\t"
    "v_mfma_f32_16x16x32_bf16 v[228:231], a[172:175], v[72:75], v[228:231]\n\t"
    "s_add_u32 m0, s42, 87968\n\t"
    "ds_read_b64_tr_b16 a[244:245], v252 offset:37152\n\t"
    "v_mfma_f32_16x16x32_bf16 v[228:231], a[176:179], v[96:99], v[228:231]\n\t"
    "ds_write_b64 v253, v[212:213] offset:59136\n\t"
    "v_mfma_f32_16x16x32_bf16 v[228:231], a[180:183], v[120:123], v[228:231]\n\t"
    "v_mfma_f32_16x16x32_bf16 v[228:231], a[184:187], v[144:147], v[228:231]\n\t"
    "ds_write_b64 v253, v[214:215] offset:59144\n\t"
    "v_mfma_f32_16x16x32_bf16 v[228:231], a[188:191], v[168:171], v[228:231]\n\t"
    "v_mfma_f32_16x16x32_bf16 v[192:195], a[128:131], v[4:7], 0\n\t"
    "ds_read_b64_tr_b16 a[246:247], v252 offset:37664\n\t"
    "v_mfma_f32_16x16x32_bf16 v[192:195], a[132:135], v[28:31], v[192:195]\n\t"
    "ds_read_b64_tr_b16 a[216:217], v252 offset:45568\n\t"
    "v_mfma_f32_16x16x32_bf16 v[192:195], a[136:139], v[52:55], v[192:195]\n\t"
    "buffer_load_dwordx4 v250, %3, s53 offen offset:896  lds\n\t"
    "v_mfma_f32_16x16x32_bf16 v[192:195], a[140:143], v[76:79], v[192:195]\n\t"
    "ds_read_b64_tr_b16 a[218:219], v252 offset:46080\n\t"
    "v_mfma_f32_16x16x32_bf16 v[192:195], a[144:147], v[100:103], v[192:195]\n\t"
    "ds_write_b64 v253, v[228:229] offset:60160\n\t"
    "v_mfma_f32_16x16x32_bf16 v[192:195], a[148:151], v[124:127], v[192:195]\n\t"
    "v_mfma_f32_16x16x32_bf16 v[192:195], a[152:155], v[148:151], v[192:195]\n\t"
    "ds_read_b64_tr_b16 a[248:249], v252 offset:45600\n\t"
    "v_mfma_f32_16x16x32_bf16 v[192:195], a[156:159], v[172:175], v[192:195]\n\t"
    "ds_write_b64 v253, v[230:231] offset:60168\n\t"
    "v_mfma_f32_16x16x32_bf16 v[236:239], a[160:163], v[4:7], 0\n\t"
    "v_mfma_f32_16x16x32_bf16 v[236:239], a[164:167], v[28:31], v[236:239]\n\t"
    "ds_read_b64_tr_b16 a[250:251], v252 offset:46112\n\t"
    "v_mfma_f32_16x16x32_bf16 v[236:239], a[168:171], v[52:55], v[236:239]\n\t"
    "ds_read_b64_tr_b16 a[220:221], v252 offset:28672\n\t"
    "v_mfma_f32_16x16x32_bf16 v[236:239], a[172:175], v[76:79], v[236:239]\n\t"
    "ds_read_b64_tr_b16 a[222:223], v252 offset:29184\n\t"
    "v_mfma_f32_16x16x32_bf16 v[236:239], a[176:179], v[100:103], v[236:239]\n\t"
    "ds_read_b64_tr_b16 a[252:253], v252 offset:28704\n\t"
    "v_mfma_f32_16x16x32_bf16 v[236:239], a[180:183], v[124:127], v[236:239]\n\t"
    "ds_write_b64 v253, v[192:193] offset:61184\n\t"
    "v_mfma_f32_16x16x32_bf16 v[236:239], a[184:187], v[148:151], v[236:239]\n\t"
    "v_mfma_f32_16x16x32_bf16 v[236:239], a[188:191], v[172:175], v[236:239]\n\t"
    "ds_read_b64_tr_b16 a[254:255], v252 offset:29216\n\t"
    "v_add_u32_e32 v251, 0xfffe7400, v251\n\t"
    "v_mfma_f32_16x16x32_bf16 v[240:243], a[128:131], v[8:11], 0\n\t"
    "ds_write_b64 v253, v[194:195] offset:61192\n\t"
    "v_mfma_f32_16x16x32_bf16 v[240:243], a[132:135], v[32:35], v[240:243]\n\t"
    "v_mfma_f32_16x16x32_bf16 v[240:243], a[136:139], v[56:59], v[240:243]\n\t"
    "ds_read_b64_tr_b16 a[0:1], v251 offset:37120\n\t"
    "v_mfma_f32_16x16x32_bf16 v[240:243], a[140:143], v[80:83], v[240:243]\n\t"
    "ds_read_b64_tr_b16 a[2:3], v251 offset:37632\n\t"
    "v_mfma_f32_16x16x32_bf16 v[240:243], a[144:147], v[104:107], v[240:243]\n\t"
    "ds_write_b64 v253, v[236:237] offset:62208\n\t"
    "v_mfma_f32_16x16x32_bf16 v[240:243], a[148:151], v[128:131], v[240:243]\n\t"
    "v_mfma_f32_16x16x32_bf16 v[240:243], a[152:155], v[152:155], v[240:243]\n\t"
    "ds_read_b64_tr_b16 a[32:33], v251 offset:37152\n\t"
    "v_mfma_f32_16x16x32_bf16 v[240:243], a[156:159], v[176:179], v[240:243]\n\t"
    "ds_write_b64 v253, v[238:239] offset:62216\n\t"
    "v_mfma_f32_16x16x32_bf16 v[224:227], a[160:163], v[8:11], 0\n\t"
    "v_mfma_f32_16x16x32_bf16 v[224:227], a[164:167], v[32:35], v[224:227]\n\t"
    "ds_read_b64_tr_b16 a[34:35], v251 offset:37664\n\t"
    "v_mfma_f32_16x16x32_bf16 v[224:227], a[168:171], v[56:59], v[224:227]\n\t"
    "v_mfma_f32_16x16x32_bf16 v[224:227], a[172:175], v[80:83], v[224:227]\n\t"
    "v_mfma_f32_16x16x32_bf16 v[224:227], a[176:179], v[104:107], v[224:227]\n\t"
    "s_nop 0\n\t"
    "ds_write_b64 v253, v[240:241] offset:63232\n\t"
    "v_mfma_f32_16x16x32_bf16 v[224:227], a[180:183], v[128:131], v[224:227]\n\t"
    "v_mfma_f32_16x16x32_bf16 v[224:227], a[184:187], v[152:155], v[224:227]\n\t"
    "ds_write_b64 v253, v[242:243] offset:63240\n\t"
    "v_mfma_f32_16x16x32_bf16 v[224:227], a[188:191], v[176:179], v[224:227]\n\t"
    "s_waitcnt vmcnt(12) lgkmcnt(8)\n\t"
    "s_barrier\n\t"
    "v_add_u32_e32 v252, 0xfffe7400, v252\n\t"
    "v_mfma_f32_16x16x32_bf16 v[216:219], a[128:131], v[12:15], 0\n\t"
    "ds_read_b64_tr_b16 a[64:65], v252 offset:37120\n\t"
    "v_mfma_f32_16x16x32_bf16 v[216:219], a[132:135], v[36:39], v[216:219]\n\t"
    "v_mfma_f32_16x16x32_bf16 v[216:219], a[136:139], v[60:63], v[216:219]\n\t"
    "ds_read_b64_tr_b16 a[66:67], v252 offset:37632\n\t"
    "v_mfma_f32_16x16x32_bf16 v[216:219], a[140:143], v[84:87], v[216:219]\n\t"
    "ds_write_b64 v253, v[224:225] offset:64256\n\t"
    "v_mfma_f32_16x16x32_bf16 v[216:219], a[144:147], v[108:111], v[216:219]\n\t"
    "v_mfma_f32_16x16x32_bf16 v[216:219], a[148:151], v[132:135], v[216:219]\n\t"
    "ds_write_b64 v253, v[226:227] offset:64264\n\t"
    "v_mfma_f32_16x16x32_bf16 v[216:219], a[152:155], v[156:159], v[216:219]\n\t"
    "v_mfma_f32_16x16x32_bf16 v[216:219], a[156:159], v[180:183], v[216:219]\n\t"
    "ds_read_b64_tr_b16 a[96:97], v252 offset:37152\n\t"
    "v_mfma_f32_16x16x32_bf16 v[212:215], a[160:163], v[12:15], 0\n\t"
    "ds_read_b64_tr_b16 a[98:99], v252 offset:37664\n\t"
    "v_mfma_f32_16x16x32_bf16 v[212:215], a[164:167], v[36:39], v[212:215]\n\t"
    "ds_read_b64_tr_b16 a[4:5], v251 offset:20224\n\t"
    "v_mfma_f32_16x16x32_bf16 v[212:215], a[168:171], v[60:63], v[212:215]\n\t"
    "ds_read_b64_tr_b16 a[6:7], v251 offset:20736\n\t"
    "v_mfma_f32_16x16x32_bf16 v[212:215], a[172:175], v[84:87], v[212:215]\n\t"
    "ds_read_b64_tr_b16 a[36:37], v251 offset:20256\n\t"
    "v_mfma_f32_16x16x32_bf16 v[212:215], a[176:179], v[108:111], v[212:215]\n\t"
    "ds_write_b64 v253, v[216:217] offset:0\n\t"
    "v_mfma_f32_16x16x32_bf16 v[212:215], a[180:183], v[132:135], v[212:215]\n\t"
    "v_mfma_f32_16x16x32_bf16 v[212:215], a[184:187], v[156:159], v[212:215]\n\t"
    "ds_write_b64 v253, v[218:219] offset:8\n\t"
    "v_mfma_f32_16x16x32_bf16 v[212:215], a[188:191], v[180:183], v[212:215]\n\t"
    "v_mfma_f32_16x16x32_bf16 v[228:231], a[128:131], v[16:19], 0\n\t"
    "ds_read_b64_tr_b16 a[38:39], v251 offset:20768\n\t"
    "v_mfma_f32_16x16x32_bf16 v[228:231], a[132:135], v[40:43], v[228:231]\n\t"
    "ds_read_b64_tr_b16 a[68:69], v252 offset:20224\n\t"
    "v_mfma_f32_16x16x32_bf16 v[228:231], a[136:139], v[64:67], v[228:231]\n\t"
    "ds_read_b64_tr_b16 a[70:71], v252 offset:20736\n\t"
    "v_mfma_f32_16x16x32_bf16 v[228:231], a[140:143], v[88:91], v[228:231]\n\t"
    "ds_read_b64_tr_b16 a[100:101], v252 offset:20256\n\t"
    "v_mfma_f32_16x16x32_bf16 v[228:231], a[144:147], v[112:115], v[228:231]\n\t"
    "ds_write_b64 v253, v[212:213] offset:1024\n\t"
    "v_mfma_f32_16x16x32_bf16 v[228:231], a[148:151], v[136:139], v[228:231]\n\t"
    "v_mfma_f32_16x16x32_bf16 v[228:231], a[152:155], v[160:163], v[228:231]\n\t"
    "ds_write_b64 v253, v[214:215] offset:1032\n\t"
    "v_mfma_f32_16x16x32_bf16 v[228:231], a[156:159], v[184:187], v[228:231]\n\t"
    "v_mfma_f32_16x16x32_bf16 v[192:195], a[160:163], v[16:19], 0\n\t"
    "ds_read_b64_tr_b16 a[102:103], v252 offset:20768\n\t"
    "v_mfma_f32_16x16x32_bf16 v[192:195], a[164:167], v[40:43], v[192:195]\n\t"
    "ds_read_b64_tr_b16 a[8:9], v251 offset:28672\n\t"
    "v_mfma_f32_16x16x32_bf16 v[192:195], a[168:171], v[64:67], v[192:195]\n\t"
    "ds_read_b64_tr_b16 a[10:11], v251 offset:29184\n\t"
    "v_mfma_f32_16x16x32_bf16 v[192:195], a[172:175], v[88:91], v[192:195]\n\t"
    "ds_read_b64_tr_b16 a[40:41], v251 offset:28704\n\t"
    "v_mfma_f32_16x16x32_bf16 v[192:195], a[176:179], v[112:115], v[192:195]\n\t"
    "ds_write_b64 v253, v[228:229] offset:2048\n\t"
    "v_mfma_f32_16x16x32_bf16 v[192:195], a[180:183], v[136:139], v[192:195]\n\t"
    "s_add_u32 m0, s42, 95872\n\t"
    "v_mfma_f32_16x16x32_bf16 v[192:195], a[184:187], v[160:163], v[192:195]\n\t"
    "ds_read_b128 v[236:239], v254 offset:59136\n\t"
    "v_mfma_f32_16x16x32_bf16 v[192:195], a[188:191], v[184:187], v[192:195]\n\t"
    "ds_write_b64 v253, v[230:231] offset:2056\n\t"
    "v_mfma_f32_16x16x32_bf16 v[240:243], a[128:131], v[20:23], 0\n\t"
    "v_mfma_f32_16x16x32_bf16 v[240:243], a[132:135], v[44:47], v[240:243]\n\t"
    "ds_read_b64_tr_b16 a[42:43], v251 offset:29216\n\t"
    "v_mfma_f32_16x16x32_bf16 v[240:243], a[136:139], v[68:71], v[240:243]\n\t"
    "ds_read_b64_tr_b16 a[72:73], v252 offset:28672\n\t"
    "v_mfma_f32_16x16x32_bf16 v[240:243], a[140:143], v[92:95], v[240:243]\n\t"
    "ds_read_b64_tr_b16 a[74:75], v252 offset:29184\n\t"
    "v_mfma_f32_16x16x32_bf16 v[240:243], a[144:147], v[116:119], v[240:243]\n\t"
    "buffer_load_dwordx4 v250, %3, s55 offen offset:384  lds\n\t"
    "v_mfma_f32_16x16x32_bf16 v[240:243], a[148:151], v[140:143], v[240:243]\n\t"
    "ds_write_b64 v253, v[192:193] offset:3072\n\t"
    "v_mfma_f32_16x16x32_bf16 v[240:243], a[152:155], v[164:167], v[240:243]\n\t"
    "s_add_u32 m0, s42, 96416\n\t"
    "v_mfma_f32_16x16x32_bf16 v[240:243], a[156:159], v[188:191], v[240:243]\n\t"
    "ds_read_b64_tr_b16 a[104:105], v252 offset:28704\n\t"
    "ds_read_b128 v[224:227], v254 offset:60160\n\t"
    "v_mfma_f32_16x16x32_bf16 v[216:219], a[160:163], v[20:23], 0\n\t"
    "ds_write_b64 v253, v[194:195] offset:3080\n\t"
    "v_mfma_f32_16x16x32_bf16 v[216:219], a[164:167], v[44:47], v[216:219]\n\t"
    "v_mfma_f32_16x16x32_bf16 v[216:219], a[168:171], v[68:71], v[216:219]\n\t"
    "ds_read_b64_tr_b16 a[106:107], v252 offset:29216\n\t"
    "v_mfma_f32_16x16x32_bf16 v[216:219], a[172:175], v[92:95], v[216:219]\n\t"
    "buffer_load_dwordx4 v250, %3, s55 offen offset:896  lds\n\t"
    "v_mfma_f32_16x16x32_bf16 v[216:219], a[176:179], v[116:119], v[216:219]\n\t"
    "ds_write_b64 v253, v[240:241] offset:4096\n\t"
    "v_mfma_f32_16x16x32_bf16 v[216:219], a[180:183], v[140:143], v[216:219]\n\t"
    "v_mfma_f32_16x16x32_bf16 v[216:219], a[184:187], v[164:167], v[216:219]\n\t"
    "ds_write_b64 v253, v[242:243] offset:4104\n\t"
    "v_mfma_f32_16x16x32_bf16 v[216:219], a[188:191], v[188:191], v[216:219]\n\t"
    "s_waitcnt vmcnt(8) lgkmcnt(2)\n\t"
    "s_barrier\n\t"
    "ds_read_b128 v[212:215], v254 offset:61184\n\t"
    "v_mfma_f32_16x16x32_bf16 v[236:239], a[192:195], v[0:3], v[236:239]\n\t"
    "s_add_u32 m0, s42, 138112\n\t"
    "v_add_u32_e32 v251, 0x25200, v251\n\t"
    "v_mfma_f32_16x16x32_bf16 v[236:239], a[196:199], v[24:27], v[236:239]\n\t"
    "buffer_load_dwordx4 v250, %3, s57 offen offset:384  lds\n\t"
    "v_mfma_f32_16x16x32_bf16 v[236:239], a[200:203], v[48:51], v[236:239]\n\t"
    "ds_read_b128 v[228:231], v254 offset:62208\n\t"
    "v_mfma_f32_16x16x32_bf16 v[236:239], a[204:207], v[72:75], v[236:239]\n\t"
    "ds_write_b64 v253, v[216:217] offset:5120\n\t"
    "v_mfma_f32_16x16x32_bf16 v[236:239], a[208:211], v[96:99], v[236:239]\n\t"
    "s_add_u32 m0, s42, 138656\n\t"
    "v_mfma_f32_16x16x32_bf16 v[236:239], a[212:215], v[120:123], v[236:239]\n\t"
    "ds_read_b64_tr_b16 a[12:13], v251 offset:3328\n\t"
    "v_mfma_f32_16x16x32_bf16 v[236:239], a[216:219], v[144:147], v[236:239]\n\t"
    "ds_write_b64 v253, v[218:219] offset:5128\n\t"
    "v_mfma_f32_16x16x32_bf16 v[236:239], a[220:223], v[168:171], v[236:239]\n\t"
    "buffer_load_dwordx4 v250, %3, s57 offen offset:896  lds\n\t"
    "v_mfma_f32_16x16x32_bf16 v[224:227], a[224:227], v[0:3], v[224:227]\n\t"
    "s_add_u32 m0, s42, 146560\n\t"
    "ds_read_b64_tr_b16 a[14:15], v251 offset:3840\n\t"
    "v_mfma_f32_16x16x32_bf16 v[224:227], a[228:231], v[24:27], v[224:227]\n\t"
    "ds_read_b64_tr_b16 a[44:45], v251 offset:3360\n\t"
    "v_mfma_f32_16x16x32_bf16 v[224:227], a[232:235], v[48:51], v[224:227]\n\t"
    "buffer_load_dwordx4 v250, %3, s59 offen offset:384  lds\n\t"
    "v_mfma_f32_16x16x32_bf16 v[224:227], a[236:239], v[72:75], v[224:227]\n\t"
    "s_add_u32 m0, s42, 147104\n\t"
    "ds_read_b128 v[192:195], v254 offset:63232\n\t"
    "v_mfma_f32_16x16x32_bf16 v[224:227], a[240:243], v[96:99], v[224:227]\n\t"
    "ds_read_b64_tr_b16 a[46:47], v251 offset:3872\n\t"
    "v_mul_f32_e32 v236, v236, v244\n\t"
    "v_mfma_f32_16x16x32_bf16 v[224:227], a[244:247], v[120:123], v[224:227]\n\t"
    "v_mul_f32_e32 v237, v237, v244\n\t"
    "v_mul_f32_e32 v238, v238, v244\n\t"
    "v_mfma_f32_16x16x32_bf16 v[224:227], a[248:251], v[144:147], v[224:227]\n\t"
    "buffer_load_dwordx4 v250, %3, s59 offen offset:896  lds\n\t"
    "v_mul_f32_e32 v239, v239, v244\n\t"
    "v_mfma_f32_16x16x32_bf16 v[224:227], a[252:255], v[168:171], v[224:227]\n\t"
    "s_add_u32 m0, s42, 129664\n\t"
    "v_add_u32_e32 v251, 0xfffdae00, v251\n\t"
    "v_cvt_pk_bf16_f32 v236, v236, v237\n\t"
    "s_waitcnt lgkmcnt(6)\n\t"
    "v_mfma_f32_16x16x32_bf16 v[212:215], a[192:195], v[4:7], v[212:215]\n\t"
    "ds_read_b64_tr_b16 a[16:17], v251 offset:3328\n\t"
    "v_cvt_pk_bf16_f32 v237, v238, v239\n\t"
    "v_mfma_f32_16x16x32_bf16 v[212:215], a[196:199], v[28:31], v[212:215]\n\t"
    "buffer_load_dwordx4 v250, %3, s61 offen offset:384  lds\n\t"
    "v_mfma_f32_16x16x32_bf16 v[212:215], a[200:203], v[52:55], v[212:215]\n\t"
    "ds_read_b128 v[240:243], v254 offset:64256\n\t"
    "v_mfma_f32_16x16x32_bf16 v[212:215], a[204:207], v[76:79], v[212:215]\n\t"
    "s_add_u32 m0, s42, 130208\n\t"
    "ds_read_b64_tr_b16 a[18:19], v251 offset:3840\n\t"
    "v_mfma_f32_16x16x32_bf16 v[212:215], a[208:211], v[100:103], v[212:215]\n\t"
    "v_mul_f32_e32 v224, v224, v244\n\t"
    "v_mul_f32_e32 v225, v225, v244\n\t"
    "v_mfma_f32_16x16x32_bf16 v[212:215], a[212:215], v[124:127], v[212:215]\n\t"
    "buffer_load_dwordx4 v250, %3, s61 offen offset:896  lds\n\t"
    "v_mul_f32_e32 v226, v226, v244\n\t"
    "v_mfma_f32_16x16x32_bf16 v[212:215], a[216:219], v[148:151], v[212:215]\n\t"
    "ds_read_b64_tr_b16 a[48:49], v251 offset:3360\n\t"
    "v_mul_f32_e32 v227, v227, v244\n\t"
    "v_mfma_f32_16x16x32_bf16 v[212:215], a[220:223], v[172:175], v[212:215]\n\t"
    "s_add_u32 m0, s42, 36736\n\t"
    "v_cvt_pk_bf16_f32 v238, v224, v225\n\t"
    "v_cvt_pk_bf16_f32 v239, v226, v227\n\t"
    "v_mfma_f32_16x16x32_bf16 v[228:231], a[224:227], v[4:7], v[228:231]\n\t"
    "ds_read_b64_tr_b16 a[50:51], v251 offset:3872\n\t"
    "v_mfma_f32_16x16x32_bf16 v[228:231], a[228:231], v[28:31], v[228:231]\n\t"
    "buffer_load_dwordx4 v250, %3, s63 offen offset:384  lds\n\t"
    "v_mfma_f32_16x16x32_bf16 v[228:231], a[232:235], v[52:55], v[228:231]\n\t"
    "v_permlane16_swap_b32_e32 v236, v238\n\t"
    "v_mfma_f32_16x16x32_bf16 v[228:231], a[236:239], v[76:79], v[228:231]\n\t"
    "s_add_u32 m0, s42, 37280\n\t"
    "v_permlane16_swap_b32_e32 v237, v239\n\t"
    "v_mfma_f32_16x16x32_bf16 v[228:231], a[240:243], v[100:103], v[228:231]\n\t"
    "ds_read_b128 v[216:219], v254 offset:0\n\t"
    "v_mul_f32_e32 v212, v212, v245\n\t"
    "v_mfma_f32_16x16x32_bf16 v[228:231], a[244:247], v[124:127], v[228:231]\n\t"
    "ds_read_b64_tr_b16 a[20:21], v251 offset:11776\n\t"
    "v_mul_f32_e32 v213, v213, v245\n\t"
    "v_mfma_f32_16x16x32_bf16 v[228:231], a[248:251], v[148:151], v[228:231]\n\t"
    "buffer_load_dwordx4 v250, %3, s63 offen offset:896  lds\n\t"
    "v_mul_f32_e32 v214, v214, v245\n\t"
    "v_mfma_f32_16x16x32_bf16 v[228:231], a[252:255], v[172:175], v[228:231]\n\t"
    "s_add_u32 m0, s42, 19840\n\t"
    "v_mul_f32_e32 v215, v215, v245\n\t"
    "v_cvt_pk_bf16_f32 v212, v212, v213\n\t"
    "s_waitcnt lgkmcnt(4)\n\t"
    "v_mfma_f32_16x16x32_bf16 v[192:195], a[192:195], v[8:11], v[192:195]\n\t"
    "ds_read_b64_tr_b16 a[22:23], v251 offset:12288\n\t"
    "v_cvt_pk_bf16_f32 v213, v214, v215\n\t"
    "v_mfma_f32_16x16x32_bf16 v[192:195], a[196:199], v[32:35], v[192:195]\n\t"
    "buffer_load_dwordx4 v250, %3, s65 offen offset:384  lds\n\t"
    "v_mfma_f32_16x16x32_bf16 v[192:195], a[200:203], v[56:59], v[192:195]\n\t"
    "ds_read_b64_tr_b16 a[52:53], v251 offset:11808\n\t"
    "v_mfma_f32_16x16x32_bf16 v[192:195], a[204:207], v[80:83], v[192:195]\n\t"
    "s_add_u32 m0, s42, 20384\n\t"
    "ds_read_b128 v[224:227], v254 offset:1024\n\t"
    "v_mfma_f32_16x16x32_bf16 v[192:195], a[208:211], v[104:107], v[192:195]\n\t"
    "v_mul_f32_e32 v228, v228, v245\n\t"
    "v_mul_f32_e32 v229, v229, v245\n\t"
    "v_mfma_f32_16x16x32_bf16 v[192:195], a[212:215], v[128:131], v[192:195]\n\t"
    "ds_read_b64_tr_b16 a[54:55], v251 offset:12320\n\t"
    "v_mul_f32_e32 v230, v230, v245\n\t"
    "v_mfma_f32_16x16x32_bf16 v[192:195], a[216:219], v[152:155], v[192:195]\n\t"
    "buffer_store_dwordx4 v[196:199], v255, %4, %8 offen offset:0 nt sc1\n\t"
    "v_mfma_f32_16x16x32_bf16 v[192:195], a[220:223], v[176:179], v[192:195]\n\t"
    "buffer_load_dwordx4 v250, %3, s65 offen offset:896  lds\n\t"
    "v_mul_f32_e32 v231, v231, v245\n\t"
    "v_mfma_f32_16x16x32_bf16 v[240:243], a[224:227], v[8:11], v[240:243]\n\t"
    "s_add_u32 m0, s42, 28288\n\t"
    "v_cvt_pk_bf16_f32 v214, v228, v229\n\t"
    "v_cvt_pk_bf16_f32 v215, v230, v231\n\t"
    "v_mfma_f32_16x16x32_bf16 v[240:243], a[228:231], v[32:35], v[240:243]\n\t"
    "buffer_load_dwordx4 v250, %3, s67 offen offset:384  lds\n\t"
    "v_mfma_f32_16x16x32_bf16 v[240:243], a[232:235], v[56:59], v[240:243]\n\t"
    "v_permlane16_swap_b32_e32 v212, v214\n\t"
    "v_mfma_f32_16x16x32_bf16 v[240:243], a[236:239], v[80:83], v[240:243]\n\t"
    "s_add_u32 m0, s42, 28832\n\t"
    "v_permlane16_swap_b32_e32 v213, v215\n\t"
    "v_mfma_f32_16x16x32_bf16 v[240:243], a[240:243], v[104:107], v[240:243]\n\t"
    "ds_read_b128 v[196:199], v254 offset:2048\n\t"
    "v_mul_f32_e32 v192, v192, v246\n\t"
    "v_mfma_f32_16x16x32_bf16 v[240:243], a[244:247], v[128:131], v[240:243]\n\t"
    "buffer_load_dwordx4 v250, %3, s67 offen offset:896  lds\n\t"
    "v_mul_f32_e32 v193, v193, v246\n\t"
    "v_mfma_f32_16x16x32_bf16 v[240:243], a[248:251], v[152:155], v[240:243]\n\t"
    "v_mul_f32_e32 v194, v194, v246\n\t"
    "v_mul_f32_e32 v195, v195, v246\n\t"
    "v_mfma_f32_16x16x32_bf16 v[240:243], a[252:255], v[176:179], v[240:243]\n\t"
    "s_waitcnt vmcnt(15)\n\t"
    "s_barrier\n\t"
    "v_cvt_pk_bf16_f32 v192, v192, v193\n\t"
    "s_waitcnt lgkmcnt(1)\n\t"
    "v_mfma_f32_16x16x32_bf16 v[216:219], a[192:195], v[12:15], v[216:219]\n\t"
    "v_cvt_pk_bf16_f32 v193, v194, v195\n\t"
    "v_add_u32_e32 v251, 0xc600, v251\n\t"
    "v_mfma_f32_16x16x32_bf16 v[216:219], a[196:199], v[36:39], v[216:219]\n\t"
    "ds_read_b128 v[228:231], v254 offset:3072\n\t"
    "v_mfma_f32_16x16x32_bf16 v[216:219], a[200:203], v[60:63], v[216:219]\n\t"
    "ds_read_b64_tr_b16 a[24:25], v251 offset:20224\n\t"
    "v_mfma_f32_16x16x32_bf16 v[216:219], a[204:207], v[84:87], v[216:219]\n\t"
    "v_mul_f32_e32 v240, v240, v246\n\t"
    "v_mul_f32_e32 v241, v241, v246\n\t"
    "v_mfma_f32_16x16x32_bf16 v[216:219], a[208:211], v[108:111], v[216:219]\n\t"
    "v_mul_f32_e32 v242, v242, v246\n\t"
    "v_mul_f32_e32 v243, v243, v246\n\t"
    "v_mfma_f32_16x16x32_bf16 v[216:219], a[212:215], v[132:135], v[216:219]\n\t"
    "buffer_store_dwordx4 v[204:207], v255, %4, s48 offen offset:0 nt sc1\n\t"
    "v_mfma_f32_16x16x32_bf16 v[216:219], a[216:219], v[156:159], v[216:219]\n\t"
    "ds_read_b64_tr_b16 a[26:27], v251 offset:20736\n\t"
    "v_cvt_pk_bf16_f32 v194, v240, v241\n\t"
    "v_mfma_f32_16x16x32_bf16 v[216:219], a[220:223], v[180:183], v[216:219]\n\t"
    "ds_read_b64_tr_b16 a[56:57], v251 offset:20256\n\t"
    "v_cvt_pk_bf16_f32 v195, v242, v243\n\t"
    "v_mfma_f32_16x16x32_bf16 v[224:227], a[224:227], v[12:15], v[224:227]\n\t"
    "v_permlane16_swap_b32_e32 v192, v194\n\t"
    "v_mfma_f32_16x16x32_bf16 v[224:227], a[228:231], v[36:39], v[224:227]\n\t"
    "v_permlane16_swap_b32_e32 v193, v195\n\t"
    "v_mfma_f32_16x16x32_bf16 v[224:227], a[232:235], v[60:63], v[224:227]\n\t"
    "ds_read_b128 v[204:207], v254 offset:4096\n\t"
    "v_mfma_f32_16x16x32_bf16 v[224:227], a[236:239], v[84:87], v[224:227]\n\t"
    "ds_read_b64_tr_b16 a[58:59], v251 offset:20768\n\t"
    "v_mfma_f32_16x16x32_bf16 v[224:227], a[240:243], v[108:111], v[224:227]\n\t"
    "v_mul_f32_e32 v216, v216, v247\n\t"
    "v_mul_f32_e32 v217, v217, v247\n\t"
    "v_mfma_f32_16x16x32_bf16 v[224:227], a[244:247], v[132:135], v[224:227]\n\t"
    "v_mul_f32_e32 v218, v218, v247\n\t"
    "v_mul_f32_e32 v219, v219, v247\n\t"
    "v_mfma_f32_16x16x32_bf16 v[224:227], a[248:251], v[156:159], v[224:227]\n\t"
    "ds_read_b64_tr_b16 a[28:29], v251 offset:28672\n\t"
    "v_cvt_pk_bf16_f32 v216, v216, v217\n\t"
    "v_mfma_f32_16x16x32_bf16 v[224:227], a[252:255], v[180:183], v[224:227]\n\t"
    "s_waitcnt lgkmcnt(5)\n\t"
    "v_cvt_pk_bf16_f32 v217, v218, v219\n\t"
    "v_mfma_f32_16x16x32_bf16 v[196:199], a[192:195], v[16:19], v[196:199]\n\t"
    "ds_read_b64_tr_b16 a[30:31], v251 offset:29184\n\t"
    "v_mfma_f32_16x16x32_bf16 v[196:199], a[196:199], v[40:43], v[196:199]\n\t"
    "ds_read_b128 v[240:243], v254 offset:5120\n\t"
    "v_mfma_f32_16x16x32_bf16 v[196:199], a[200:203], v[64:67], v[196:199]\n\t"
    "ds_read_b64_tr_b16 a[60:61], v251 offset:28704\n\t"
    "v_mfma_f32_16x16x32_bf16 v[196:199], a[204:207], v[88:91], v[196:199]\n\t"
    "v_mfma_f32_16x16x32_bf16 v[196:199], a[208:211], v[112:115], v[196:199]\n\t"
    "v_mul_f32_e32 v224, v224, v247\n\t"
    "v_mul_f32_e32 v225, v225, v247\n\t"
    "v_mfma_f32_16x16x32_bf16 v[196:199], a[212:215], v[136:139], v[196:199]\n\t"
    "buffer_store_dwordx4 v[220:223], v255, %4, s49 offen offset:0 nt sc1\n\t"
    "v_mfma_f32_16x16x32_bf16 v[196:199], a[216:219], v[160:163], v[196:199]\n\t"
    "v_mul_f32_e32 v226, v226, v247\n\t"
    "v_mul_f32_e32 v227, v227, v247\n\t"
    "v_mfma_f32_16x16x32_bf16 v[196:199], a[220:223], v[184:187], v[196:199]\n\t"
    "ds_read_b64_tr_b16 a[62:63], v251 offset:29216\n\t"
    "v_cvt_pk_bf16_f32 v218, v224, v225\n\t"
    "v_mfma_f32_16x16x32_bf16 v[228:231], a[224:227], v[16:19], v[228:231]\n\t"
    "v_cvt_pk_bf16_f32 v219, v226, v227\n\t"
    "v_add_u32_e32 v252, 0x25200, v252\n\t"
    "v_mfma_f32_16x16x32_bf16 v[228:231], a[228:231], v[40:43], v[228:231]\n\t"
    "v_permlane16_swap_b32_e32 v216, v218\n\t"
    "v_mfma_f32_16x16x32_bf16 v[228:231], a[232:235], v[64:67], v[228:231]\n\t"
    "v_permlane16_swap_b32_e32 v217, v219\n\t"
    "v_mfma_f32_16x16x32_bf16 v[228:231], a[236:239], v[88:91], v[228:231]\n\t"
    "ds_read_b64_tr_b16 a[76:77], v252 offset:3328\n\t"
    "v_mfma_f32_16x16x32_bf16 v[228:231], a[240:243], v[112:115], v[228:231]\n\t"
    "v_mul_f32_e32 v196, v196, v248\n\t"
    "v_mul_f32_e32 v197, v197, v248\n\t"
    "v_mfma_f32_16x16x32_bf16 v[228:231], a[244:247], v[136:139], v[228:231]\n\t"
    "v_mul_f32_e32 v198, v198, v248\n\t"
    "v_mul_f32_e32 v199, v199, v248\n\t"
    "v_mfma_f32_16x16x32_bf16 v[228:231], a[248:251], v[160:163], v[228:231]\n\t"
    "ds_read_b64_tr_b16 a[78:79], v252 offset:3840\n\t"
    "v_cvt_pk_bf16_f32 v196, v196, v197\n\t"
    "v_mfma_f32_16x16x32_bf16 v[228:231], a[252:255], v[184:187], v[228:231]\n\t"
    "s_waitcnt lgkmcnt(3)\n\t"
    "v_cvt_pk_bf16_f32 v197, v198, v199\n\t"
    "v_mfma_f32_16x16x32_bf16 v[204:207], a[192:195], v[20:23], v[204:207]\n\t"
    "ds_read_b64_tr_b16 a[108:109], v252 offset:3360\n\t"
    "v_mfma_f32_16x16x32_bf16 v[204:207], a[196:199], v[44:47], v[204:207]\n\t"
    "v_mfma_f32_16x16x32_bf16 v[204:207], a[200:203], v[68:71], v[204:207]\n\t"
    "v_mfma_f32_16x16x32_bf16 v[204:207], a[204:207], v[92:95], v[204:207]\n\t"
    "ds_read_b64_tr_b16 a[110:111], v252 offset:3872\n\t"
    "v_mfma_f32_16x16x32_bf16 v[204:207], a[208:211], v[116:119], v[204:207]\n\t"
    "v_mul_f32_e32 v228, v228, v248\n\t"
    "v_mul_f32_e32 v229, v229, v248\n\t"
    "v_mfma_f32_16x16x32_bf16 v[204:207], a[212:215], v[140:143], v[204:207]\n\t"
    "buffer_store_dwordx4 v[208:211], v255, %4, s50 offen offset:0 nt sc1\n\t"
    "v_mfma_f32_16x16x32_bf16 v[204:207], a[216:219], v[164:167], v[204:207]\n\t"
    "v_mul_f32_e32 v230, v230, v248\n\t"
    "v_mul_f32_e32 v231, v231, v248\n\t"
    "v_mfma_f32_16x16x32_bf16 v[204:207], a[220:223], v[188:191], v[204:207]\n\t"
    "v_cvt_pk_bf16_f32 v198, v228, v229\n\t"
    "v_cvt_pk_bf16_f32 v199, v230, v231\n\t"
    "v_mfma_f32_16x16x32_bf16 v[240:243], a[224:227], v[20:23], v[240:243]\n\t"
    "v_permlane16_swap_b32_e32 v196, v198\n\t"
    "v_mfma_f32_16x16x32_bf16 v[240:243], a[228:231], v[44:47], v[240:243]\n\t"
    "v_permlane16_swap_b32_e32 v197, v199\n\t"
    "v_mfma_f32_16x16x32_bf16 v[240:243], a[232:235], v[68:71], v[240:243]\n\t"
    "v_mfma_f32_16x16x32_bf16 v[240:243], a[236:239], v[92:95], v[240:243]\n\t"
    "v_mfma_f32_16x16x32_bf16 v[240:243], a[240:243], v[116:119], v[240:243]\n\t"
    "v_mul_f32_e32 v204, v204, v249\n\t"
    "v_mul_f32_e32 v205, v205, v249\n\t"
    "v_mfma_f32_16x16x32_bf16 v[240:243], a[244:247], v[140:143], v[240:243]\n\t"
    "v_mul_f32_e32 v206, v206, v249\n\t"
    "v_mul_f32_e32 v207, v207, v249\n\t"
    "v_mfma_f32_16x16x32_bf16 v[240:243], a[248:251], v[164:167], v[240:243]\n\t"
    "v_cvt_pk_bf16_f32 v204, v204, v205\n\t"
    "v_cvt_pk_bf16_f32 v205, v206, v207\n\t"
    "v_mfma_f32_16x16x32_bf16 v[240:243], a[252:255], v[188:191], v[240:243]\n\t"
    "v_add_u32_e32 v252, 0xfffdae00, v252\n\t"
    "v_mfma_f32_16x16x32_bf16 v[220:223], a[0:3], v[0:3], 0\n\t"
    "v_mfma_f32_16x16x32_bf16 v[220:223], a[4:7], v[24:27], v[220:223]\n\t"
    "v_mfma_f32_16x16x32_bf16 v[220:223], a[8:11], v[48:51], v[220:223]\n\t"
    "ds_read_b64_tr_b16 a[80:81], v252 offset:3328\n\t"
    "v_mfma_f32_16x16x32_bf16 v[220:223], a[12:15], v[72:75], v[220:223]\n\t"
    "v_mfma_f32_16x16x32_bf16 v[220:223], a[16:19], v[96:99], v[220:223]\n\t"
    "s_nop 0\n\t"
    "v_mul_f32_e32 v240, v240, v249\n\t"
    "v_mul_f32_e32 v241, v241, v249\n\t"
    "v_mfma_f32_16x16x32_bf16 v[220:223], a[20:23], v[120:123], v[220:223]\n\t"
    "buffer_store_dwordx4 v[232:235], v255, %4, s51 offen offset:0 nt sc1\n\t"
    "v_mfma_f32_16x16x32_bf16 v[220:223], a[24:27], v[144:147], v[220:223]\n\t"
    "ds_read_b64_tr_b16 a[82:83], v252 offset:3840\n\t"
    "v_mul_f32_e32 v242, v242, v249\n\t"
    "v_mfma_f32_16x16x32_bf16 v[220:223], a[28:31], v[168:171], v[220:223]\n\t"
    "v_mul_f32_e32 v243, v243, v249\n\t"
    "v_cvt_pk_bf16_f32 v206, v240, v241\n\t"
    "v_mfma_f32_16x16x32_bf16 v[224:227], a[32:35], v[0:3], 0\n\t"
    "ds_read_b64_tr_b16 a[112:113], v252 offset:3360\n\t"
    "v_cvt_pk_bf16_f32 v207, v242, v243\n\t"
    "v_mfma_f32_16x16x32_bf16 v[224:227], a[36:39], v[24:27], v[224:227]\n\t"
    "v_permlane16_swap_b32_e32 v204, v206\n\t"
    "v_mfma_f32_16x16x32_bf16 v[224:227], a[40:43], v[48:51], v[224:227]\n\t"
    "v_permlane16_swap_b32_e32 v205, v207\n\t"
    "v_mfma_f32_16x16x32_bf16 v[224:227], a[44:47], v[72:75], v[224:227]\n\t"
    "ds_read_b64_tr_b16 a[114:115], v252 offset:3872\n\t"
    "v_mfma_f32_16x16x32_bf16 v[224:227], a[48:51], v[96:99], v[224:227]\n\t"
    "ds_write_b64 v253, v[220:221] offset:59136\n\t"
    "v_mfma_f32_16x16x32_bf16 v[224:227], a[52:55], v[120:123], v[224:227]\n\t"
    "v_mfma_f32_16x16x32_bf16 v[224:227], a[56:59], v[144:147], v[224:227]\n\t"
    "s_waitcnt lgkmcnt(4)\n\t"
    "ds_write_b64 v253, v[222:223] offset:59144\n\t"
    "v_mfma_f32_16x16x32_bf16 v[224:227], a[60:63], v[168:171], v[224:227]\n\t"
    "v_mfma_f32_16x16x32_bf16 v[208:211], a[0:3], v[4:7], 0\n\t"
    "ds_read_b64_tr_b16 a[84:85], v252 offset:11776\n\t"
    "v_mfma_f32_16x16x32_bf16 v[208:211], a[4:7], v[28:31], v[208:211]\n\t"
    "ds_read_b64_tr_b16 a[86:87], v252 offset:12288\n\t"
    "v_mfma_f32_16x16x32_bf16 v[208:211], a[8:11], v[52:55], v[208:211]\n\t"
    "v_mfma_f32_16x16x32_bf16 v[208:211], a[12:15], v[76:79], v[208:211]\n\t"
    "v_mfma_f32_16x16x32_bf16 v[208:211], a[16:19], v[100:103], v[208:211]\n\t"
    "s_nop 0\n\t"
    "ds_write_b64 v253, v[224:225] offset:60160\n\t"
    "v_mfma_f32_16x16x32_bf16 v[208:211], a[20:23], v[124:127], v[208:211]\n\t"
    "buffer_store_dwordx4 v[200:203], v255, %4, s52 offen offset:0 nt sc1\n\t"
    "v_mfma_f32_16x16x32_bf16 v[208:211], a[24:27], v[148:151], v[208:211]\n\t"
    "ds_write_b64 v253, v[226:227] offset:60168\n\t"
    "v_mfma_f32_16x16x32_bf16 v[208:211], a[28:31], v[172:175], v[208:211]\n\t"
    "v_mfma_f32_16x16x32_bf16 v[228:231], a[32:35], v[4:7], 0\n\t"
    "ds_read_b64_tr_b16 a[116:117], v252 offset:11808\n\t"
    "v_mfma_f32_16x16x32_bf16 v[228:231], a[36:39], v[28:31], v[228:231]\n\t"
    "ds_read_b64_tr_b16 a[118:119], v252 offset:12320\n\t"
    "v_mfma_f32_16x16x32_bf16 v[228:231], a[40:43], v[52:55], v[228:231]\n\t"
    "ds_read_b64_tr_b16 a[128:129], v251 offset:37120\n\t"
    "v_mfma_f32_16x16x32_bf16 v[228:231], a[44:47], v[76:79], v[228:231]\n\t"
    "v_mfma_f32_16x16x32_bf16 v[228:231], a[48:51], v[100:103], v[228:231]\n\t"
    "ds_write_b64 v253, v[208:209] offset:61184\n\t"
    "v_mfma_f32_16x16x32_bf16 v[228:231], a[52:55], v[124:127], v[228:231]\n\t"
    "v_mfma_f32_16x16x32_bf16 v[228:231], a[56:59], v[148:151], v[228:231]\n\t"
    "ds_write_b64 v253, v[210:211] offset:61192\n\t"
    "v_mfma_f32_16x16x32_bf16 v[228:231], a[60:63], v[172:175], v[228:231]\n\t"
    "v_mfma_f32_16x16x32_bf16 v[232:235], a[0:3], v[8:11], 0\n\t"
    "ds_read_b64_tr_b16 a[130:131], v251 offset:37632\n\t"
    "v_mfma_f32_16x16x32_bf16 v[232:235], a[4:7], v[32:35], v[232:235]\n\t"
    "ds_read_b64_tr_b16 a[160:161], v251 offset:37152\n\t"
    "v_mfma_f32_16x16x32_bf16 v[232:235], a[8:11], v[56:59], v[232:235]\n\t"
    "v_mfma_f32_16x16x32_bf16 v[232:235], a[12:15], v[80:83], v[232:235]\n\t"
    "ds_read_b64_tr_b16 a[162:163], v251 offset:37664\n\t"
    "v_mfma_f32_16x16x32_bf16 v[232:235], a[16:19], v[104:107], v[232:235]\n\t"
    "ds_write_b64 v253, v[228:229] offset:62208\n\t"
    "v_mfma_f32_16x16x32_bf16 v[232:235], a[20:23], v[128:131], v[232:235]\n\t"
    "buffer_store_dwordx4 v[236:239], v255, %4, %8 offen offset:128 nt sc1\n\t"
    "v_mfma_f32_16x16x32_bf16 v[232:235], a[24:27], v[152:155], v[232:235]\n\t"
    "PG6_%=:\n\t"
    "s_cbranch_execz RET_%=\n\t"
    "ds_write_b64 v253, v[230:231] offset:62216\n\t"
    "v_mfma_f32_16x16x32_bf16 v[232:235], a[28:31], v[176:179], v[232:235]\n\t"
    "v_mfma_f32_16x16x32_bf16 v[240:243], a[32:35], v[8:11], 0\n\t"
    "v_mfma_f32_16x16x32_bf16 v[240:243], a[36:39], v[32:35], v[240:243]\n\t"
    "v_mfma_f32_16x16x32_bf16 v[240:243], a[40:43], v[56:59], v[240:243]\n\t"
    "v_mfma_f32_16x16x32_bf16 v[240:243], a[44:47], v[80:83], v[240:243]\n\t"
    "v_mfma_f32_16x16x32_bf16 v[240:243], a[48:51], v[104:107], v[240:243]\n\t"
    "s_nop 2\n\t"
    "ds_write_b64 v253, v[232:233] offset:63232\n\t"
    "v_mfma_f32_16x16x32_bf16 v[240:243], a[52:55], v[128:131], v[240:243]\n\t"
    "v_mfma_f32_16x16x32_bf16 v[240:243], a[56:59], v[152:155], v[240:243]\n\t"
    "ds_write_b64 v253, v[234:235] offset:63240\n\t"
    "v_mfma_f32_16x16x32_bf16 v[240:243], a[60:63], v[176:179], v[240:243]\n\t"
    "s_waitcnt vmcnt(19) lgkmcnt(5)\n\t"
    "s_barrier\n\t"
    "v_add_u32_e32 v252, 0xc600, v252\n\t"
    "v_mfma_f32_16x16x32_bf16 v[220:223], a[0:3], v[12:15], 0\n\t"
    "v_mfma_f32_16x16x32_bf16 v[220:223], a[4:7], v[36:39], v[220:223]\n\t"
    "v_mfma_f32_16x16x32_bf16 v[220:223], a[8:11], v[60:63], v[220:223]\n\t"
    "ds_read_b64_tr_b16 a[88:89], v252 offset:20224\n\t"
    "v_mfma_f32_16x16x32_bf16 v[220:223], a[12:15], v[84:87], v[220:223]\n\t"
    "ds_write_b64 v253, v[240:241] offset:64256\n\t"
    "v_mfma_f32_16x16x32_bf16 v[220:223], a[16:19], v[108:111], v[220:223]\n\t"
    "buffer_store_dwordx4 v[212:215], v255, %4, s48 offen offset:128 nt sc1\n\t"
    "v_mfma_f32_16x16x32_bf16 v[220:223], a[20:23], v[132:135], v[220:223]\n\t"
    "ds_write_b64 v253, v[242:243] offset:64264\n\t"
    "v_mfma_f32_16x16x32_bf16 v[220:223], a[24:27], v[156:159], v[220:223]\n\t"
    "v_mfma_f32_16x16x32_bf16 v[220:223], a[28:31], v[180:183], v[220:223]\n\t"
    "ds_read_b64_tr_b16 a[90:91], v252 offset:20736\n\t"
    "v_mfma_f32_16x16x32_bf16 v[200:203], a[32:35], v[12:15], 0\n\t"
    "ds_read_b64_tr_b16 a[120:121], v252 offset:20256\n\t"
    "v_mfma_f32_16x16x32_bf16 v[200:203], a[36:39], v[36:39], v[200:203]\n\t"
    "v_mfma_f32_16x16x32_bf16 v[200:203], a[40:43], v[60:63], v[200:203]\n\t"
    "v_mfma_f32_16x16x32_bf16 v[200:203], a[44:47], v[84:87], v[200:203]\n\t"
    "ds_read_b64_tr_b16 a[122:123], v252 offset:20768\n\t"
    "v_mfma_f32_16x16x32_bf16 v[200:203], a[48:51], v[108:111], v[200:203]\n\t"
    "ds_write_b64 v253, v[220:221] offset:0\n\t"
    "v_mfma_f32_16x16x32_bf16 v[200:203], a[52:55], v[132:135], v[200:203]\n\t"
    "v_mfma_f32_16x16x32_bf16 v[200:203], a[56:59], v[156:159], v[200:203]\n\t"
    "ds_write_b64 v253, v[222:223] offset:8\n\t"
    "v_mfma_f32_16x16x32_bf16 v[200:203], a[60:63], v[180:183], v[200:203]\n\t"
    "v_mfma_f32_16x16x32_bf16 v[224:227], a[0:3], v[16:19], 0\n\t"
    "ds_read_b64_tr_b16 a[92:93], v252 offset:28672\n\t"
    "v_mfma_f32_16x16x32_bf16 v[224:227], a[4:7], v[40:43], v[224:227]\n\t"
    "ds_read_b64_tr_b16 a[94:95], v252 offset:29184\n\t"
    "v_mfma_f32_16x16x32_bf16 v[224:227], a[8:11], v[64:67], v[224:227]\n\t"
    "v_mfma_f32_16x16x32_bf16 v[224:227], a[12:15], v[88:91], v[224:227]\n\t"
    "v_mfma_f32_16x16x32_bf16 v[224:227], a[16:19], v[112:115], v[224:227]\n\t"
    "s_nop 0\n\t"
    "ds_write_b64 v253, v[200:201] offset:1024\n\t"
    "v_mfma_f32_16x16x32_bf16 v[224:227], a[20:23], v[136:139], v[224:227]\n\t"
    "buffer_store_dwordx4 v[192:195], v255, %4, s49 offen offset:128 nt sc1\n\t"
    "v_mfma_f32_16x16x32_bf16 v[224:227], a[24:27], v[160:163], v[224:227]\n\t"
    "ds_write_b64 v253, v[202:203] offset:1032\n\t"
    "v_mfma_f32_16x16x32_bf16 v[224:227], a[28:31], v[184:187], v[224:227]\n\t"
    "v_mfma_f32_16x16x32_bf16 v[208:211], a[32:35], v[16:19], 0\n\t"
    "ds_read_b64_tr_b16 a[124:125], v252 offset:28704\n\t"
    "v_mfma_f32_16x16x32_bf16 v[208:211], a[36:39], v[40:43], v[208:211]\n\t"
    "ds_read_b64_tr_b16 a[126:127], v252 offset:29216\n\t"
    "v_mfma_f32_16x16x32_bf16 v[208:211], a[40:43], v[64:67], v[208:211]\n\t"
    "ds_read_b64_tr_b16 a[132:133], v251 offset:45568\n\t"
    "v_mfma_f32_16x16x32_bf16 v[208:211], a[44:47], v[88:91], v[208:211]\n\t"
    "ds_read_b128 v[236:239], v254 offset:59136\n\t"
    "v_mfma_f32_16x16x32_bf16 v[208:211], a[48:51], v[112:115], v[208:211]\n\t"
    "ds_write_b64 v253, v[224:225] offset:2048\n\t"
    "v_mfma_f32_16x16x32_bf16 v[208:211], a[52:55], v[136:139], v[208:211]\n\t"
    "v_mfma_f32_16x16x32_bf16 v[208:211], a[56:59], v[160:163], v[208:211]\n\t"
    "ds_write_b64 v253, v[226:227] offset:2056\n\t"
    "v_mfma_f32_16x16x32_bf16 v[208:211], a[60:63], v[184:187], v[208:211]\n\t"
    "v_mfma_f32_16x16x32_bf16 v[228:231], a[0:3], v[20:23], 0\n\t"
    "ds_read_b64_tr_b16 a[134:135], v251 offset:46080\n\t"
    "v_mfma_f32_16x16x32_bf16 v[228:231], a[4:7], v[44:47], v[228:231]\n\t"
    "ds_read_b64_tr_b16 a[164:165], v251 offset:45600\n\t"
    "v_mfma_f32_16x16x32_bf16 v[228:231], a[8:11], v[68:71], v[228:231]\n\t"
    "ds_read_b128 v[232:235], v254 offset:60160\n\t"
    "v_mfma_f32_16x16x32_bf16 v[228:231], a[12:15], v[92:95], v[228:231]\n\t"
    "ds_read_b64_tr_b16 a[166:167], v251 offset:46112\n\t"
    "v_mfma_f32_16x16x32_bf16 v[228:231], a[16:19], v[116:119], v[228:231]\n\t"
    "ds_write_b64 v253, v[208:209] offset:3072\n\t"
    "v_mfma_f32_16x16x32_bf16 v[228:231], a[20:23], v[140:143], v[228:231]\n\t"
    "buffer_store_dwordx4 v[216:219], v255, %4, s50 offen offset:128 nt sc1\n\t"
    "v_mfma_f32_16x16x32_bf16 v[228:231], a[24:27], v[164:167], v[228:231]\n\t"
    "ds_write_b64 v253, v[210:211] offset:3080\n\t"
    "v_mfma_f32_16x16x32_bf16 v[228:231], a[28:31], v[188:191], v[228:231]\n\t"
    "v_mfma_f32_16x16x32_bf16 v[212:215], a[32:35], v[20:23], 0\n\t"
    "v_mfma_f32_16x16x32_bf16 v[212:215], a[36:39], v[44:47], v[212:215]\n\t"
    "v_mfma_f32_16x16x32_bf16 v[212:215], a[40:43], v[68:71], v[212:215]\n\t"
    "ds_read_b128 v[240:243], v254 offset:61184\n\t"
    "v_mfma_f32_16x16x32_bf16 v[212:215], a[44:47], v[92:95], v[212:215]\n\t"
    "v_mfma_f32_16x16x32_bf16 v[212:215], a[48:51], v[116:119], v[212:215]\n\t"
    "s_nop 1\n\t"
    "ds_write_b64 v253, v[228:229] offset:4096\n\t"
    "v_mfma_f32_16x16x32_bf16 v[212:215], a[52:55], v[140:143], v[212:215]\n\t"
    "v_mfma_f32_16x16x32_bf16 v[212:215], a[56:59], v[164:167], v[212:215]\n\t"
    "ds_write_b64 v253, v[230:231] offset:4104\n\t"
    "v_mfma_f32_16x16x32_bf16 v[212:215], a[60:63], v[188:191], v[212:215]\n\t"
    "s_waitcnt vmcnt(16) lgkmcnt(6)\n\t"
    "s_barrier\n\t"
    "v_add_u32_e32 v251, 0xc600, v251\n\t"
    "v_mfma_f32_16x16x32_bf16 v[236:239], a[64:67], v[0:3], v[236:239]\n\t"
    "v_mfma_f32_16x16x32_bf16 v[236:239], a[68:71], v[24:27], v[236:239]\n\t"
    "ds_read_b128 v[220:223], v254 offset:62208\n\t"
    "v_mfma_f32_16x16x32_bf16 v[236:239], a[72:75], v[48:51], v[236:239]\n\t"
    "ds_read_b64_tr_b16 a[136:137], v251 offset:37120\n\t"
    "v_mfma_f32_16x16x32_bf16 v[236:239], a[76:79], v[72:75], v[236:239]\n\t"
    "ds_write_b64 v253, v[212:213] offset:5120\n\t"
    "v_mfma_f32_16x16x32_bf16 v[236:239], a[80:83], v[96:99], v[236:239]\n\t"
    "buffer_store_dwordx4 v[196:199], v255, %4, s51 offen offset:128 nt sc1\n\t"
    "v_mfma_f32_16x16x32_bf16 v[236:239], a[84:87], v[120:123], v[236:239]\n\t"
    "ds_write_b64 v253, v[214:215] offset:5128\n\t"
    "v_mfma_f32_16x16x32_bf16 v[236:239], a[88:91], v[144:147], v[236:239]\n\t"
    "v_mfma_f32_16x16x32_bf16 v[236:239], a[92:95], v[168:171], v[236:239]\n\t"
    "ds_read_b64_tr_b16 a[138:139], v251 offset:37632\n\t"
    "v_mfma_f32_16x16x32_bf16 v[232:235], a[96:99], v[0:3], v[232:235]\n\t"
    "ds_read_b64_tr_b16 a[168:169], v251 offset:37152\n\t"
    "v_mfma_f32_16x16x32_bf16 v[232:235], a[100:103], v[24:27], v[232:235]\n\t"
    "ds_read_b128 v[192:195], v254 offset:63232\n\t"
    "v_mfma_f32_16x16x32_bf16 v[232:235], a[104:107], v[48:51], v[232:235]\n\t"
    "v_mfma_f32_16x16x32_bf16 v[232:235], a[108:111], v[72:75], v[232:235]\n\t"
    "ds_read_b64_tr_b16 a[170:171], v251 offset:37664\n\t"
    "v_mfma_f32_16x16x32_bf16 v[232:235], a[112:115], v[96:99], v[232:235]\n\t"
    "v_mul_f32_e32 v236, v236, v244\n\t"
    "v_mul_f32_e32 v237, v237, v244\n\t"
    "v_mfma_f32_16x16x32_bf16 v[232:235], a[116:119], v[120:123], v[232:235]\n\t"
    "v_mul_f32_e32 v238, v238, v244\n\t"
    "v_mul_f32_e32 v239, v239, v244\n\t"
    "v_mfma_f32_16x16x32_bf16 v[232:235], a[120:123], v[144:147], v[232:235]\n\t"
    "ds_read_b64_tr_b16 a[140:141], v251 offset:45568\n\t"
    "v_cvt_pk_bf16_f32 v236, v236, v237\n\t"
    "v_mfma_f32_16x16x32_bf16 v[232:235], a[124:127], v[168:171], v[232:235]\n\t"
    "s_waitcnt lgkmcnt(6)\n\t"
    "v_cvt_pk_bf16_f32 v237, v238, v239\n\t"
    "v_mfma_f32_16x16x32_bf16 v[240:243], a[64:67], v[4:7], v[240:243]\n\t"
    "v_mfma_f32_16x16x32_bf16 v[240:243], a[68:71], v[28:31], v[240:243]\n\t"
    "ds_read_b64_tr_b16 a[142:143], v251 offset:46080\n\t"
    "v_mfma_f32_16x16x32_bf16 v[240:243], a[72:75], v[52:55], v[240:243]\n\t"
    "ds_read_b128 v[200:203], v254 offset:64256\n\t"
    "v_mfma_f32_16x16x32_bf16 v[240:243], a[76:79], v[76:79], v[240:243]\n\t"
    "buffer_store_dwordx4 v[204:207], v255, %4, s52 offen offset:128 nt sc1\n\t"
    "v_mfma_f32_16x16x32_bf16 v[240:243], a[80:83], v[100:103], v[240:243]\n\t"
    "v_mul_f32_e32 v232, v232, v244\n\t"
    "v_mul_f32_e32 v233, v233, v244\n\t"
    "v_mfma_f32_16x16x32_bf16 v[240:243], a[84:87], v[124:127], v[240:243]\n\t"
    "ds_read_b64_tr_b16 a[172:173], v251 offset:45600\n\t"
    "v_mul_f32_e32 v234, v234, v244\n\t"
    "v_mfma_f32_16x16x32_bf16 v[240:243], a[88:91], v[148:151], v[240:243]\n\t"
    "v_mul_f32_e32 v235, v235, v244\n\t"
    "v_cvt_pk_bf16_f32 v238, v232, v233\n\t"
    "v_mfma_f32_16x16x32_bf16 v[240:243], a[92:95], v[172:175], v[240:243]\n\t"
    "ds_read_b64_tr_b16 a[174:175], v251 offset:46112\n\t"
    "v_cvt_pk_bf16_f32 v239, v234, v235\n\t"
    "v_mfma_f32_16x16x32_bf16 v[220:223], a[96:99], v[4:7], v[220:223]\n\t"
    "v_permlane16_swap_b32_e32 v236, v238\n\t"
    "v_mfma_f32_16x16x32_bf16 v[220:223], a[100:103], v[28:31], v[220:223]\n\t"
    "v_permlane16_swap_b32_e32 v237, v239\n\t"
    "v_mfma_f32_16x16x32_bf16 v[220:223], a[104:107], v[52:55], v[220:223]\n\t"
    "ds_read_b64_tr_b16 a[144:145], v251 offset:28672\n\t"
    "v_mfma_f32_16x16x32_bf16 v[220:223], a[108:111], v[76:79], v[220:223]\n\t"
    "ds_read_b128 v[224:227], v254 offset:0\n\t"
    "v_mfma_f32_16x16x32_bf16 v[220:223], a[112:115], v[100:103], v[220:223]\n\t"
    "v_mul_f32_e32 v240, v240, v245\n\t"
    "v_mul_f32_e32 v241, v241, v245\n\t"
    "v_mfma_f32_16x16x32_bf16 v[220:223], a[116:119], v[124:127], v[220:223]\n\t"
    "v_mul_f32_e32 v242, v242, v245\n\t"
    "v_mul_f32_e32 v243, v243, v245\n\t"
    "v_mfma_f32_16x16x32_bf16 v[220:223], a[120:123], v[148:151], v[220:223]\n\t"
    "ds_read_b64_tr_b16 a[146:147], v251 offset:29184\n\t"
    "v_cvt_pk_bf16_f32 v240, v240, v241\n\t"
    "v_mfma_f32_16x16x32_bf16 v[220:223], a[124:127], v[172:175], v[220:223]\n\t"
    "s_waitcnt lgkmcnt(5)\n\t"
    "v_cvt_pk_bf16_f32 v241, v242, v243\n\t"
    "v_mfma_f32_16x16x32_bf16 v[192:195], a[64:67], v[8:11], v[192:195]\n\t"
    "ds_read_b64_tr_b16 a[176:177], v251 offset:28704\n\t"
    "v_mfma_f32_16x16x32_bf16 v[192:195], a[68:71], v[32:35], v[192:195]\n\t"
    "v_mfma_f32_16x16x32_bf16 v[192:195], a[72:75], v[56:59], v[192:195]\n\t"
    "ds_read_b128 v[216:219], v254 offset:1024\n\t"
    "v_mfma_f32_16x16x32_bf16 v[192:195], a[76:79], v[80:83], v[192:195]\n\t"
    "buffer_store_dwordx4 v[236:239], v255, %4, %8 offen offset:256 nt sc1\n\t"
    "v_mfma_f32_16x16x32_bf16 v[192:195], a[80:83], v[104:107], v[192:195]\n\t"
    "ds_read_b64_tr_b16 a[178:179], v251 offset:29216\n\t"
    "v_mul_f32_e32 v220, v220, v245\n\t"
    "v_mfma_f32_16x16x32_bf16 v[192:195], a[84:87], v[128:131], v[192:195]\n\t"
    "v_mul_f32_e32 v221, v221, v245\n\t"
    "v_mul_f32_e32 v222, v222, v245\n\t"
    "v_mfma_f32_16x16x32_bf16 v[192:195], a[88:91], v[152:155], v[192:195]\n\t"
    "v_mul_f32_e32 v223, v223, v245\n\t"
    "v_cvt_pk_bf16_f32 v242, v220, v221\n\t"
    "v_mfma_f32_16x16x32_bf16 v[192:195], a[92:95], v[176:179], v[192:195]\n\t"
    "v_cvt_pk_bf16_f32 v243, v222, v223\n\t"
    "v_mfma_f32_16x16x32_bf16 v[200:203], a[96:99], v[8:11], v[200:203]\n\t"
    "v_permlane16_swap_b32_e32 v240, v242\n\t"
    "v_mfma_f32_16x16x32_bf16 v[200:203], a[100:103], v[32:35], v[200:203]\n\t"
    "v_permlane16_swap_b32_e32 v241, v243\n\t"
    "v_mfma_f32_16x16x32_bf16 v[200:203], a[104:107], v[56:59], v[200:203]\n\t"
    "ds_read_b128 v[208:211], v254 offset:2048\n\t"
    "v_mfma_f32_16x16x32_bf16 v[200:203], a[108:111], v[80:83], v[200:203]\n\t"
    "v_mfma_f32_16x16x32_bf16 v[200:203], a[112:115], v[104:107], v[200:203]\n\t"
    "v_mul_f32_e32 v192, v192, v246\n\t"
    "v_mul_f32_e32 v193, v193, v246\n\t"
    "v_mfma_f32_16x16x32_bf16 v[200:203], a[116:119], v[128:131], v[200:203]\n\t"
    "v_mul_f32_e32 v194, v194, v246\n\t"
    "v_mul_f32_e32 v195, v195, v246\n\t"
    "v_mfma_f32_16x16x32_bf16 v[200:203], a[120:123], v[152:155], v[200:203]\n\t"
    "v_cvt_pk_bf16_f32 v192, v192, v193\n\t"
    "v_cvt_pk_bf16_f32 v193, v194, v195\n\t"
    "v_mfma_f32_16x16x32_bf16 v[200:203], a[124:127], v[176:179], v[200:203]\n\t"
    "s_waitcnt vmcnt(12)\n\t"
    "s_barrier\n\t"
    "v_add_u32_e32 v251, 0xfffe7400, v251\n\t"
    "s_waitcnt lgkmcnt(2)\n\t"
    "v_mfma_f32_16x16x32_bf16 v[224:227], a[64:67], v[12:15], v[224:227]\n\t"
    "v_mfma_f32_16x16x32_bf16 v[224:227], a[68:71], v[36:39], v[224:227]\n\t"
    "ds_read_b128 v[228:231], v254 offset:3072\n\t"
    "v_mfma_f32_16x16x32_bf16 v[224:227], a[72:75], v[60:63], v[224:227]\n\t"
    "buffer_store_dwordx4 v[240:243], v255, %4, s48 offen offset:256 nt sc1\n\t"
    "v_mfma_f32_16x16x32_bf16 v[224:227], a[76:79], v[84:87], v[224:227]\n\t"
    "ds_read_b64_tr_b16 a[148:149], v251 offset:37120\n\t"
    "v_mul_f32_e32 v200, v200, v246\n\t"
    "v_mfma_f32_16x16x32_bf16 v[224:227], a[80:83], v[108:111], v[224:227]\n\t"
    "v_mul_f32_e32 v201, v201, v246\n\t"
    "v_mul_f32_e32 v202, v202, v246\n\t"
    "v_mfma_f32_16x16x32_bf16 v[224:227], a[84:87], v[132:135], v[224:227]\n\t"
    "ds_read_b64_tr_b16 a[150:151], v251 offset:37632\n\t"
    "v_mul_f32_e32 v203, v203, v246\n\t"
    "v_mfma_f32_16x16x32_bf16 v[224:227], a[88:91], v[156:159], v[224:227]\n\t"
    "v_cvt_pk_bf16_f32 v194, v200, v201\n\t"
    "v_cvt_pk_bf16_f32 v195, v202, v203\n\t"
    "v_mfma_f32_16x16x32_bf16 v[224:227], a[92:95], v[180:183], v[224:227]\n\t"
    "v_permlane16_swap_b32_e32 v192, v194\n\t"
    "v_mfma_f32_16x16x32_bf16 v[216:219], a[96:99], v[12:15], v[216:219]\n\t"
    "ds_read_b64_tr_b16 a[180:181], v251 offset:37152\n\t"
    "v_mfma_f32_16x16x32_bf16 v[216:219], a[100:103], v[36:39], v[216:219]\n\t"
    "v_permlane16_swap_b32_e32 v193, v195\n\t"
    "v_mfma_f32_16x16x32_bf16 v[216:219], a[104:107], v[60:63], v[216:219]\n\t"
    "ds_read_b128 v[196:199], v254 offset:4096\n\t"
    "v_mfma_f32_16x16x32_bf16 v[216:219], a[108:111], v[84:87], v[216:219]\n\t"
    "ds_read_b64_tr_b16 a[182:183], v251 offset:37664\n\t"
    "v_mfma_f32_16x16x32_bf16 v[216:219], a[112:115], v[108:111], v[216:219]\n\t"
    "v_mul_f32_e32 v224, v224, v247\n\t"
    "v_mul_f32_e32 v225, v225, v247\n\t"
    "v_mfma_f32_16x16x32_bf16 v[216:219], a[116:119], v[132:135], v[216:219]\n\t"
    "v_mul_f32_e32 v226, v226, v247\n\t"
    "v_mul_f32_e32 v227, v227, v247\n\t"
    "v_mfma_f32_16x16x32_bf16 v[216:219], a[120:123], v[156:159], v[216:219]\n\t"
    "ds_read_b64_tr_b16 a[152:153], v251 offset:20224\n\t"
    "v_cvt_pk_bf16_f32 v224, v224, v225\n\t"
    "v_mfma_f32_16x16x32_bf16 v[216:219], a[124:127], v[180:183], v[216:219]\n\t"
    "s_waitcnt lgkmcnt(5)\n\t"
    "v_cvt_pk_bf16_f32 v225, v226, v227\n\t"
    "v_mfma_f32_16x16x32_bf16 v[208:211], a[64:67], v[16:19], v[208:211]\n\t"
    "v_mfma_f32_16x16x32_bf16 v[208:211], a[68:71], v[40:43], v[208:211]\n\t"
    "ds_read_b64_tr_b16 a[154:155], v251 offset:20736\n\t"
    "v_mfma_f32_16x16x32_bf16 v[208:211], a[72:75], v[64:67], v[208:211]\n\t"
    "buffer_store_dwordx4 v[192:195], v255, %4, s49 offen offset:256 nt sc1\n\t"
    "v_mfma_f32_16x16x32_bf16 v[208:211], a[76:79], v[88:91], v[208:211]\n\t"
    "ds_read_b128 v[212:215], v254 offset:5120\n\t"
    "v_mfma_f32_16x16x32_bf16 v[208:211], a[80:83], v[112:115], v[208:211]\n\t"
    "v_mul_f32_e32 v216, v216, v247\n\t"
    "v_mul_f32_e32 v217, v217, v247\n\t"
    "v_mfma_f32_16x16x32_bf16 v[208:211], a[84:87], v[136:139], v[208:211]\n\t"
    "ds_read_b64_tr_b16 a[184:185], v251 offset:20256\n\t"
    "v_mul_f32_e32 v218, v218, v247\n\t"
    "v_mfma_f32_16x16x32_bf16 v[208:211], a[88:91], v[160:163], v[208:211]\n\t"
    "v_mul_f32_e32 v219, v219, v247\n\t"
    "v_cvt_pk_bf16_f32 v226, v216, v217\n\t"
    "v_mfma_f32_16x16x32_bf16 v[208:211], a[92:95], v[184:187], v[208:211]\n\t"
    "ds_read_b64_tr_b16 a[186:187], v251 offset:20768\n\t"
    "v_cvt_pk_bf16_f32 v227, v218, v219\n\t"
    "v_mfma_f32_16x16x32_bf16 v[228:231], a[96:99], v[16:19], v[228:231]\n\t"
    "v_permlane16_swap_b32_e32 v224, v226\n\t"
    "v_mfma_f32_16x16x32_bf16 v[228:231], a[100:103], v[40:43], v[228:231]\n\t"
    "v_permlane16_swap_b32_e32 v225, v227\n\t"
    "v_mfma_f32_16x16x32_bf16 v[228:231], a[104:107], v[64:67], v[228:231]\n\t"
    "ds_read_b64_tr_b16 a[156:157], v251 offset:28672\n\t"
    "v_mfma_f32_16x16x32_bf16 v[228:231], a[108:111], v[88:91], v[228:231]\n\t"
    "v_mfma_f32_16x16x32_bf16 v[228:231], a[112:115], v[112:115], v[228:231]\n\t"
    "v_mul_f32_e32 v208, v208, v248\n\t"
    "v_mul_f32_e32 v209, v209, v248\n\t"
    "v_mfma_f32_16x16x32_bf16 v[228:231], a[116:119], v[136:139], v[228:231]\n\t"
    "v_mul_f32_e32 v210, v210, v248\n\t"
    "v_mul_f32_e32 v211, v211, v248\n\t"
    "v_mfma_f32_16x16x32_bf16 v[228:231], a[120:123], v[160:163], v[228:231]\n\t"
    "ds_read_b64_tr_b16 a[158:159], v251 offset:29184\n\t"
    "v_cvt_pk_bf16_f32 v208, v208, v209\n\t"
    "v_mfma_f32_16x16x32_bf16 v[228:231], a[124:127], v[184:187], v[228:231]\n\t"
    "s_waitcnt lgkmcnt(4)\n\t"
    "v_cvt_pk_bf16_f32 v209, v210, v211\n\t"
    "v_mfma_f32_16x16x32_bf16 v[196:199], a[64:67], v[20:23], v[196:199]\n\t"
    "ds_read_b64_tr_b16 a[188:189], v251 offset:28704\n\t"
    "v_mfma_f32_16x16x32_bf16 v[196:199], a[68:71], v[44:47], v[196:199]\n\t"
    "v_mfma_f32_16x16x32_bf16 v[196:199], a[72:75], v[68:71], v[196:199]\n\t"
    "buffer_store_dwordx4 v[224:227], v255, %4, s50 offen offset:256 nt sc1\n\t"
    "v_mfma_f32_16x16x32_bf16 v[196:199], a[76:79], v[92:95], v[196:199]\n\t"
    "ds_read_b64_tr_b16 a[190:191], v251 offset:29216\n\t"
    "v_mfma_f32_16x16x32_bf16 v[196:199], a[80:83], v[116:119], v[196:199]\n\t"
    "v_mul_f32_e32 v228, v228, v248\n\t"
    "v_mul_f32_e32 v229, v229, v248\n\t"
    "v_mfma_f32_16x16x32_bf16 v[196:199], a[84:87], v[140:143], v[196:199]\n\t"
    "v_mul_f32_e32 v230, v230, v248\n\t"
    "v_mul_f32_e32 v231, v231, v248\n\t"
    "v_mfma_f32_16x16x32_bf16 v[196:199], a[88:91], v[164:167], v[196:199]\n\t"
    "v_cvt_pk_bf16_f32 v210, v228, v229\n\t"
    "v_cvt_pk_bf16_f32 v211, v230, v231\n\t"
    "v_mfma_f32_16x16x32_bf16 v[196:199], a[92:95], v[188:191], v[196:199]\n\t"
    "v_permlane16_swap_b32_e32 v208, v210\n\t"
    "v_mfma_f32_16x16x32_bf16 v[212:215], a[96:99], v[20:23], v[212:215]\n\t"
    "v_permlane16_swap_b32_e32 v209, v211\n\t"
    "v_mfma_f32_16x16x32_bf16 v[212:215], a[100:103], v[44:47], v[212:215]\n\t"
    "v_mfma_f32_16x16x32_bf16 v[212:215], a[104:107], v[68:71], v[212:215]\n\t"
    "v_mfma_f32_16x16x32_bf16 v[212:215], a[108:111], v[92:95], v[212:215]\n\t"
    "v_mfma_f32_16x16x32_bf16 v[212:215], a[112:115], v[116:119], v[212:215]\n\t"
    "s_nop 0\n\t"
    "v_mul_f32_e32 v196, v196, v249\n\t"
    "v_mul_f32_e32 v197, v197, v249\n\t"
    "v_mfma_f32_16x16x32_bf16 v[212:215], a[116:119], v[140:143], v[212:215]\n\t"
    "v_mul_f32_e32 v198, v198, v249\n\t"
    "v_mul_f32_e32 v199, v199, v249\n\t"
    "v_mfma_f32_16x16x32_bf16 v[212:215], a[120:123], v[164:167], v[212:215]\n\t"
    "v_cvt_pk_bf16_f32 v196, v196, v197\n\t"
    "v_cvt_pk_bf16_f32 v197, v198, v199\n\t"
    "v_mfma_f32_16x16x32_bf16 v[212:215], a[124:127], v[188:191], v[212:215]\n\t"
    "ds_read_b64_tr_b16 a[192:193], v252 offset:37120\n\t"
    "v_mfma_f32_16x16x32_bf16 v[204:207], a[128:131], v[0:3], 0\n\t"
    "v_mfma_f32_16x16x32_bf16 v[204:207], a[132:135], v[24:27], v[204:207]\n\t"
    "ds_read_b64_tr_b16 a[194:195], v252 offset:37632\n\t"
    "v_mfma_f32_16x16x32_bf16 v[204:207], a[136:139], v[48:51], v[204:207]\n\t"
    "buffer_store_dwordx4 v[208:211], v255, %4, s51 offen offset:256 nt sc1\n\t"
    "v_mfma_f32_16x16x32_bf16 v[204:207], a[140:143], v[72:75], v[204:207]\n\t"
    "ds_read_b64_tr_b16 a[224:225], v252 offset:37152\n\t"
    "v_mfma_f32_16x16x32_bf16 v[204:207], a[144:147], v[96:99], v[204:207]\n\t"
    "v_mul_f32_e32 v212, v212, v249\n\t"
    "v_mul_f32_e32 v213, v213, v249\n\t"
    "v_mfma_f32_16x16x32_bf16 v[204:207], a[148:151], v[120:123], v[204:207]\n\t"
    "v_mul_f32_e32 v214, v214, v249\n\t"
    "v_mul_f32_e32 v215, v215, v249\n\t"
    "v_mfma_f32_16x16x32_bf16 v[204:207], a[152:155], v[144:147], v[204:207]\n\t"
    "s_waitcnt lgkmcnt(3)\n\t"
    "ds_read_b64_tr_b16 a[226:227], v252 offset:37664\n\t"
    "v_cvt_pk_bf16_f32 v198, v212, v213\n\t"
    "v_mfma_f32_16x16x32_bf16 v[204:207], a[156:159], v[168:171], v[204:207]\n\t"
    "v_cvt_pk_bf16_f32 v199, v214, v215\n\t"
    "v_mfma_f32_16x16x32_bf16 v[232:235], a[160:163], v[0:3], 0\n\t"
    "ds_read_b64_tr_b16 a[196:197], v252 offset:45568\n\t"
    "v_mfma_f32_16x16x32_bf16 v[232:235], a[164:167], v[24:27], v[232:235]\n\t"
    "v_permlane16_swap_b32_e32 v196, v198\n\t"
    "v_mfma_f32_16x16x32_bf16 v[232:235], a[168:171], v[48:51], v[232:235]\n\t"
    "v_permlane16_swap_b32_e32 v197, v199\n\t"
    "v_mfma_f32_16x16x32_bf16 v[232:235], a[172:175], v[72:75], v[232:235]\n\t"
    "ds_read_b64_tr_b16 a[198:199], v252 offset:46080\n\t"
    "v_mfma_f32_16x16x32_bf16 v[232:235], a[176:179], v[96:99], v[232:235]\n\t"
    "ds_write_b64 v253, v[204:205] offset:59136\n\t"
    "v_mfma_f32_16x16x32_bf16 v[232:235], a[180:183], v[120:123], v[232:235]\n\t"
    "v_mfma_f32_16x16x32_bf16 v[232:235], a[184:187], v[144:147], v[232:235]\n\t"
    "ds_write_b64 v253, v[206:207] offset:59144\n\t"
    "v_mfma_f32_16x16x32_bf16 v[232:235], a[188:191], v[168:171], v[232:235]\n\t"
    "v_mfma_f32_16x16x32_bf16 v[236:239], a[128:131], v[4:7], 0\n\t"
    "ds_read_b64_tr_b16 a[228:229], v252 offset:45600\n\t"
    "v_mfma_f32_16x16x32_bf16 v[236:239], a[132:135], v[28:31], v[236:239]\n\t"
    "ds_read_b64_tr_b16 a[230:231], v252 offset:46112\n\t"
    "v_add_u32_e32 v252, 0xc600, v252\n\t"
    "v_mfma_f32_16x16x32_bf16 v[236:239], a[136:139], v[52:55], v[236:239]\n\t"
    "buffer_store_dwordx4 v[196:199], v255, %4, s52 offen offset:256 nt sc1\n\t"
    "v_mfma_f32_16x16x32_bf16 v[236:239], a[140:143], v[76:79], v[236:239]\n\t"
    "ds_read_b64_tr_b16 a[200:201], v252 offset:37120\n\t"
    "v_mfma_f32_16x16x32_bf16 v[236:239], a[144:147], v[100:103], v[236:239]\n\t"
    "ds_write_b64 v253, v[232:233] offset:60160\n\t"
    "v_mfma_f32_16x16x32_bf16 v[236:239], a[148:151], v[124:127], v[236:239]\n\t"
    "v_mfma_f32_16x16x32_bf16 v[236:239], a[152:155], v[148:151], v[236:239]\n\t"
    "ds_write_b64 v253, v[234:235] offset:60168\n\t"
    "v_mfma_f32_16x16x32_bf16 v[236:239], a[156:159], v[172:175], v[236:239]\n\t"
    "v_mfma_f32_16x16x32_bf16 v[220:223], a[160:163], v[4:7], 0\n\t"
    "ds_read_b64_tr_b16 a[202:203], v252 offset:37632\n\t"
    "v_mfma_f32_16x16x32_bf16 v[220:223], a[164:167], v[28:31], v[220:223]\n\t"
    "ds_read_b64_tr_b16 a[232:233], v252 offset:37152\n\t"
    "v_mfma_f32_16x16x32_bf16 v[220:223], a[168:171], v[52:55], v[220:223]\n\t"
    "ds_read_b64_tr_b16 a[234:235], v252 offset:37664\n\t"
    "v_mfma_f32_16x16x32_bf16 v[220:223], a[172:175], v[76:79], v[220:223]\n\t"
    "v_mfma_f32_16x16x32_bf16 v[220:223], a[176:179], v[100:103], v[220:223]\n\t"
    "ds_write_b64 v253, v[236:237] offset:61184\n\t"
    "v_mfma_f32_16x16x32_bf16 v[220:223], a[180:183], v[124:127], v[220:223]\n\t"
    "v_mfma_f32_16x16x32_bf16 v[220:223], a[184:187], v[148:151], v[220:223]\n\t"
    "ds_write_b64 v253, v[238:239] offset:61192\n\t"
    "v_mfma_f32_16x16x32_bf16 v[220:223], a[188:191], v[172:175], v[220:223]\n\t"
    "v_mfma_f32_16x16x32_bf16 v[240:243], a[128:131], v[8:11], 0\n\t"
    "ds_read_b64_tr_b16 a[204:205], v252 offset:45568\n\t"
    "v_mfma_f32_16x16x32_bf16 v[240:243], a[132:135], v[32:35], v[240:243]\n\t"
    "ds_read_b64_tr_b16 a[206:207], v252 offset:46080\n\t"
    "v_mfma_f32_16x16x32_bf16 v[240:243], a[136:139], v[56:59], v[240:243]\n\t"
    "ds_read_b64_tr_b16 a[236:237], v252 offset:45600\n\t"
    "v_mfma_f32_16x16x32_bf16 v[240:243], a[140:143], v[80:83], v[240:243]\n\t"
    "ds_read_b64_tr_b16 a[238:239], v252 offset:46112\n\t"
    "v_mfma_f32_16x16x32_bf16 v[240:243], a[144:147], v[104:107], v[240:243]\n\t"
    "ds_write_b64 v253, v[220:221] offset:62208\n\t"
    "v_mfma_f32_16x16x32_bf16 v[240:243], a[148:151], v[128:131], v[240:243]\n\t"
    "v_mfma_f32_16x16x32_bf16 v[240:243], a[152:155], v[152:155], v[240:243]\n\t"
    "ds_write_b64 v253, v[222:223] offset:62216\n\t"
    "v_mfma_f32_16x16x32_bf16 v[240:243], a[156:159], v[176:179], v[240:243]\n\t"
    "v_mfma_f32_16x16x32_bf16 v[200:203], a[160:163], v[8:11], 0\n\t"
    "v_mfma_f32_16x16x32_bf16 v[200:203], a[164:167], v[32:35], v[200:203]\n\t"
    "v_mfma_f32_16x16x32_bf16 v[200:203], a[168:171], v[56:59], v[200:203]\n\t"
    "v_mfma_f32_16x16x32_bf16 v[200:203], a[172:175], v[80:83], v[200:203]\n\t"
    "v_mfma_f32_16x16x32_bf16 v[200:203], a[176:179], v[104:107], v[200:203]\n\t"
    "s_nop 2\n\t"
    "ds_write_b64 v253, v[240:241] offset:63232\n\t"
    "v_mfma_f32_16x16x32_bf16 v[200:203], a[180:183], v[128:131], v[200:203]\n\t"
    "v_mfma_f32_16x16x32_bf16 v[200:203], a[184:187], v[152:155], v[200:203]\n\t"
    "ds_write_b64 v253, v[242:243] offset:63240\n\t"
    "v_mfma_f32_16x16x32_bf16 v[200:203], a[188:191], v[176:179], v[200:203]\n\t"
    "s_waitcnt lgkmcnt(6)\n\t"
    "s_barrier\n\t"
    "ds_read_b64_tr_b16 a[208:209], v252 offset:28672\n\t"
    "v_mfma_f32_16x16x32_bf16 v[192:195], a[128:131], v[12:15], 0\n\t"
    "v_mfma_f32_16x16x32_bf16 v[192:195], a[132:135], v[36:39], v[192:195]\n\t"
    "ds_read_b64_tr_b16 a[210:211], v252 offset:29184\n\t"
    "v_mfma_f32_16x16x32_bf16 v[192:195], a[136:139], v[60:63], v[192:195]\n\t"
    "v_mfma_f32_16x16x32_bf16 v[192:195], a[140:143], v[84:87], v[192:195]\n\t"
    "ds_write_b64 v253, v[200:201] offset:64256\n\t"
    "v_mfma_f32_16x16x32_bf16 v[192:195], a[144:147], v[108:111], v[192:195]\n\t"
    "v_mfma_f32_16x16x32_bf16 v[192:195], a[148:151], v[132:135], v[192:195]\n\t"
    "ds_write_b64 v253, v[202:203] offset:64264\n\t"
    "v_mfma_f32_16x16x32_bf16 v[192:195], a[152:155], v[156:159], v[192:195]\n\t"
    "v_mfma_f32_16x16x32_bf16 v[192:195], a[156:159], v[180:183], v[192:195]\n\t"
    "ds_read_b64_tr_b16 a[240:241], v252 offset:28704\n\t"
    "v_mfma_f32_16x16x32_bf16 v[216:219], a[160:163], v[12:15], 0\n\t"
    "ds_read_b64_tr_b16 a[242:243], v252 offset:29216\n\t"
    "v_add_u32_e32 v252, 0xfffe7400, v252\n\t"
    "v_mfma_f32_16x16x32_bf16 v[216:219], a[164:167], v[36:39], v[216:219]\n\t"
    "v_mfma_f32_16x16x32_bf16 v[216:219], a[168:171], v[60:63], v[216:219]\n\t"
    "ds_read_b64_tr_b16 a[212:213], v252 offset:37120\n\t"
    "v_mfma_f32_16x16x32_bf16 v[216:219], a[172:175], v[84:87], v[216:219]\n\t"
    "v_mfma_f32_16x16x32_bf16 v[216:219], a[176:179], v[108:111], v[216:219]\n\t"
    "ds_write_b64 v253, v[192:193] offset:0\n\t"
    "v_mfma_f32_16x16x32_bf16 v[216:219], a[180:183], v[132:135], v[216:219]\n\t"
    "v_mfma_f32_16x16x32_bf16 v[216:219], a[184:187], v[156:159], v[216:219]\n\t"
    "ds_write_b64 v253, v[194:195] offset:8\n\t"
    "v_mfma_f32_16x16x32_bf16 v[216:219], a[188:191], v[180:183], v[216:219]\n\t"
    "v_mfma_f32_16x16x32_bf16 v[224:227], a[128:131], v[16:19], 0\n\t"
    "ds_read_b64_tr_b16 a[214:215], v252 offset:37632\n\t"
    "v_mfma_f32_16x16x32_bf16 v[224:227], a[132:135], v[40:43], v[224:227]\n\t"
    "ds_read_b64_tr_b16 a[244:245], v252 offset:37152\n\t"
    "v_mfma_f32_16x16x32_bf16 v[224:227], a[136:139], v[64:67], v[224:227]\n\t"
    "ds_read_b64_tr_b16 a[246:247], v252 offset:37664\n\t"
    "v_mfma_f32_16x16x32_bf16 v[224:227], a[140:143], v[88:91], v[224:227]\n\t"
    "ds_read_b64_tr_b16 a[216:217], v252 offset:20224\n\t"
    "v_mfma_f32_16x16x32_bf16 v[224:227], a[144:147], v[112:115], v[224:227]\n\t"
    "ds_write_b64 v253, v[216:217] offset:1024\n\t"
    "v_mfma_f32_16x16x32_bf16 v[224:227], a[148:151], v[136:139], v[224:227]\n\t"
    "v_mfma_f32_16x16x32_bf16 v[224:227], a[152:155], v[160:163], v[224:227]\n\t"
    "ds_write_b64 v253, v[218:219] offset:1032\n\t"
    "v_mfma_f32_16x16x32_bf16 v[224:227], a[156:159], v[184:187], v[224:227]\n\t"
    "v_mfma_f32_16x16x32_bf16 v[228:231], a[160:163], v[16:19], 0\n\t"
    "ds_read_b64_tr_b16 a[218:219], v252 offset:20736\n\t"
    "v_mfma_f32_16x16x32_bf16 v[228:231], a[164:167], v[40:43], v[228:231]\n\t"
    "ds_read_b64_tr_b16 a[248:249], v252 offset:20256\n\t"
    "v_mfma_f32_16x16x32_bf16 v[228:231], a[168:171], v[64:67], v[228:231]\n\t"
    "ds_read_b64_tr_b16 a[250:251], v252 offset:20768\n\t"
    "v_mfma_f32_16x16x32_bf16 v[228:231], a[172:175], v[88:91], v[228:231]\n\t"
    "ds_read_b128 v[208:211], v254 offset:59136\n\t"
    "v_mfma_f32_16x16x32_bf16 v[228:231], a[176:179], v[112:115], v[228:231]\n\t"
    "ds_write_b64 v253, v[224:225] offset:2048\n\t"
    "v_mfma_f32_16x16x32_bf16 v[228:231], a[180:183], v[136:139], v[228:231]\n\t"
    "v_mfma_f32_16x16x32_bf16 v[228:231], a[184:187], v[160:163], v[228:231]\n\t"
    "ds_write_b64 v253, v[226:227] offset:2056\n\t"
    "v_mfma_f32_16x16x32_bf16 v[228:231], a[188:191], v[184:187], v[228:231]\n\t"
    "v_mfma_f32_16x16x32_bf16 v[212:215], a[128:131], v[20:23], 0\n\t"
    "ds_read_b64_tr_b16 a[220:221], v252 offset:28672\n\t"
    "v_mfma_f32_16x16x32_bf16 v[212:215], a[132:135], v[44:47], v[212:215]\n\t"
    "ds_read_b64_tr_b16 a[222:223], v252 offset:29184\n\t"
    "v_mfma_f32_16x16x32_bf16 v[212:215], a[136:139], v[68:71], v[212:215]\n\t"
    "ds_read_b64_tr_b16 a[252:253], v252 offset:28704\n\t"
    "v_mfma_f32_16x16x32_bf16 v[212:215], a[140:143], v[92:95], v[212:215]\n\t"
    "ds_read_b128 v[204:207], v254 offset:60160\n\t"
    "v_mfma_f32_16x16x32_bf16 v[212:215], a[144:147], v[116:119], v[212:215]\n\t"
    "ds_write_b64 v253, v[228:229] offset:3072\n\t"
    "v_mfma_f32_16x16x32_bf16 v[212:215], a[148:151], v[140:143], v[212:215]\n\t"
    "v_mfma_f32_16x16x32_bf16 v[212:215], a[152:155], v[164:167], v[212:215]\n\t"
    "ds_read_b64_tr_b16 a[254:255], v252 offset:29216\n\t"
    "v_mfma_f32_16x16x32_bf16 v[212:215], a[156:159], v[188:191], v[212:215]\n\t"
    "ds_write_b64 v253, v[230:231] offset:3080\n\t"
    "v_mfma_f32_16x16x32_bf16 v[196:199], a[160:163], v[20:23], 0\n\t"
    "v_mfma_f32_16x16x32_bf16 v[196:199], a[164:167], v[44:47], v[196:199]\n\t"
    "v_mfma_f32_16x16x32_bf16 v[196:199], a[168:171], v[68:71], v[196:199]\n\t"
    "ds_read_b128 v[232:235], v254 offset:61184\n\t"
    "v_mfma_f32_16x16x32_bf16 v[196:199], a[172:175], v[92:95], v[196:199]\n\t"
    "v_mfma_f32_16x16x32_bf16 v[196:199], a[176:179], v[116:119], v[196:199]\n\t"
    "s_nop 0\n\t"
    "ds_write_b64 v253, v[212:213] offset:4096\n\t"
    "v_mfma_f32_16x16x32_bf16 v[196:199], a[180:183], v[140:143], v[196:199]\n\t"
    "v_mfma_f32_16x16x32_bf16 v[196:199], a[184:187], v[164:167], v[196:199]\n\t"
    "ds_write_b64 v253, v[214:215] offset:4104\n\t"
    "v_mfma_f32_16x16x32_bf16 v[196:199], a[188:191], v[188:191], v[196:199]\n\t"
    "s_waitcnt lgkmcnt(8)\n\t"
    "s_barrier\n\t"
    "v_mfma_f32_16x16x32_bf16 v[208:211], a[192:195], v[0:3], v[208:211]\n\t"
    "v_mfma_f32_16x16x32_bf16 v[208:211], a[196:199], v[24:27], v[208:211]\n\t"
    "ds_read_b128 v[236:239], v254 offset:62208\n\t"
    "v_mfma_f32_16x16x32_bf16 v[208:211], a[200:203], v[48:51], v[208:211]\n\t"
    "v_mfma_f32_16x16x32_bf16 v[208:211], a[204:207], v[72:75], v[208:211]\n\t"
    "s_nop 0\n\t"
    "ds_write_b64 v253, v[196:197] offset:5120\n\t"
    "v_mfma_f32_16x16x32_bf16 v[208:211], a[208:211], v[96:99], v[208:211]\n\t"
    "v_mfma_f32_16x16x32_bf16 v[208:211], a[212:215], v[120:123], v[208:211]\n\t"
    "ds_write_b64 v253, v[198:199] offset:5128\n\t"
    "v_mfma_f32_16x16x32_bf16 v[208:211], a[216:219], v[144:147], v[208:211]\n\t"
    "v_mfma_f32_16x16x32_bf16 v[208:211], a[220:223], v[168:171], v[208:211]\n\t"
    "s_waitcnt lgkmcnt(6)\n\t"
    "v_mfma_f32_16x16x32_bf16 v[204:207], a[224:227], v[0:3], v[204:207]\n\t"
    "v_mfma_f32_16x16x32_bf16 v[204:207], a[228:231], v[24:27], v[204:207]\n\t"
    "ds_read_b128 v[220:223], v254 offset:63232\n\t"
    "v_mfma_f32_16x16x32_bf16 v[204:207], a[232:235], v[48:51], v[204:207]\n\t"
    "v_mfma_f32_16x16x32_bf16 v[204:207], a[236:239], v[72:75], v[204:207]\n\t"
    "v_mfma_f32_16x16x32_bf16 v[204:207], a[240:243], v[96:99], v[204:207]\n\t"
    "s_nop 0\n\t"
    "v_mul_f32_e32 v208, v208, v244\n\t"
    "v_mul_f32_e32 v209, v209, v244\n\t"
    "v_mfma_f32_16x16x32_bf16 v[204:207], a[244:247], v[120:123], v[204:207]\n\t"
    "v_mul_f32_e32 v210, v210, v244\n\t"
    "v_mul_f32_e32 v211, v211, v244\n\t"
    "v_mfma_f32_16x16x32_bf16 v[204:207], a[248:251], v[144:147], v[204:207]\n\t"
    "v_cvt_pk_bf16_f32 v208, v208, v209\n\t"
    "v_cvt_pk_bf16_f32 v209, v210, v211\n\t"
    "v_mfma_f32_16x16x32_bf16 v[204:207], a[252:255], v[168:171], v[204:207]\n\t"
    "s_waitcnt lgkmcnt(4)\n\t"
    "v_mfma_f32_16x16x32_bf16 v[232:235], a[192:195], v[4:7], v[232:235]\n\t"
    "v_mfma_f32_16x16x32_bf16 v[232:235], a[196:199], v[28:31], v[232:235]\n\t"
    "ds_read_b128 v[240:243], v254 offset:64256\n\t"
    "v_mfma_f32_16x16x32_bf16 v[232:235], a[200:203], v[52:55], v[232:235]\n\t"
    "v_mfma_f32_16x16x32_bf16 v[232:235], a[204:207], v[76:79], v[232:235]\n\t"
    "v_mfma_f32_16x16x32_bf16 v[232:235], a[208:211], v[100:103], v[232:235]\n\t"
    "s_nop 0\n\t"
    "v_mul_f32_e32 v204, v204, v244\n\t"
    "v_mul_f32_e32 v205, v205, v244\n\t"
    "v_mfma_f32_16x16x32_bf16 v[232:235], a[212:215], v[124:127], v[232:235]\n\t"
    "v_mul_f32_e32 v206, v206, v244\n\t"
    "v_mul_f32_e32 v207, v207, v244\n\t"
    "v_mfma_f32_16x16x32_bf16 v[232:235], a[216:219], v[148:151], v[232:235]\n\t"
    "v_cvt_pk_bf16_f32 v210, v204, v205\n\t"
    "v_cvt_pk_bf16_f32 v211, v206, v207\n\t"
    "v_mfma_f32_16x16x32_bf16 v[232:235], a[220:223], v[172:175], v[232:235]\n\t"
    "s_waitcnt lgkmcnt(1)\n\t"
    "v_permlane16_swap_b32_e32 v208, v210\n\t"
    "v_mfma_f32_16x16x32_bf16 v[236:239], a[224:227], v[4:7], v[236:239]\n\t"
    "v_permlane16_swap_b32_e32 v209, v211\n\t"
    "v_mfma_f32_16x16x32_bf16 v[236:239], a[228:231], v[28:31], v[236:239]\n\t"
    "v_mfma_f32_16x16x32_bf16 v[236:239], a[232:235], v[52:55], v[236:239]\n\t"
    "ds_read_b128 v[200:203], v254 offset:0\n\t"
    "v_mfma_f32_16x16x32_bf16 v[236:239], a[236:239], v[76:79], v[236:239]\n\t"
    "v_mfma_f32_16x16x32_bf16 v[236:239], a[240:243], v[100:103], v[236:239]\n\t"
    "v_mfma_f32_16x16x32_bf16 v[236:239], a[244:247], v[124:127], v[236:239]\n\t"
    "buffer_store_dwordx4 v[208:211], v255, %4, %8 offen offset:384 nt sc1\n\t"
    "v_mfma_f32_16x16x32_bf16 v[236:239], a[248:251], v[148:151], v[236:239]\n\t"
    "v_mul_f32_e32 v232, v232, v245\n\t"
    "v_mul_f32_e32 v233, v233, v245\n\t"
    "v_mul_f32_e32 v234, v234, v245\n\t"
    "v_mfma_f32_16x16x32_bf16 v[236:239], a[252:255], v[172:175], v[236:239]\n\t"
    "v_mul_f32_e32 v235, v235, v245\n\t"
    "v_cvt_pk_bf16_f32 v232, v232, v233\n\t"
    "v_mfma_f32_16x16x32_bf16 v[220:223], a[192:195], v[8:11], v[220:223]\n\t"
    "v_cvt_pk_bf16_f32 v233, v234, v235\n\t"
    "v_mfma_f32_16x16x32_bf16 v[220:223], a[196:199], v[32:35], v[220:223]\n\t"
    "v_mfma_f32_16x16x32_bf16 v[220:223], a[200:203], v[56:59], v[220:223]\n\t"
    "ds_read_b128 v[192:195], v254 offset:1024\n\t"
    "v_mfma_f32_16x16x32_bf16 v[220:223], a[204:207], v[80:83], v[220:223]\n\t"
    "PG7_%=:\n\t"
    "s_cbranch_execz RET_%=\n\t"
    "v_mfma_f32_16x16x32_bf16 v[220:223], a[208:211], v[104:107], v[220:223]\n\t"
    "v_mul_f32_e32 v236, v236, v245\n\t"
    "v_mul_f32_e32 v237, v237, v245\n\t"
    "v_mfma_f32_16x16x32_bf16 v[220:223], a[212:215], v[128:131], v[220:223]\n\t"
    "v_mul_f32_e32 v238, v238, v245\n\t"
    "v_mul_f32_e32 v239, v239, v245\n\t"
    "v_mfma_f32_16x16x32_bf16 v[220:223], a[216:219], v[152:155], v[220:223]\n\t"
    "v_cvt_pk_bf16_f32 v234, v236, v237\n\t"
    "v_cvt_pk_bf16_f32 v235, v238, v239\n\t"
    "v_mfma_f32_16x16x32_bf16 v[220:223], a[220:223], v[176:179], v[220:223]\n\t"
    "s_waitcnt lgkmcnt(1)\n\t"
    "v_permlane16_swap_b32_e32 v232, v234\n\t"
    "v_mfma_f32_16x16x32_bf16 v[240:243], a[224:227], v[8:11], v[240:243]\n\t"
    "v_permlane16_swap_b32_e32 v233, v235\n\t"
    "v_mfma_f32_16x16x32_bf16 v[240:243], a[228:231], v[32:35], v[240:243]\n\t"
    "v_mfma_f32_16x16x32_bf16 v[240:243], a[232:235], v[56:59], v[240:243]\n\t"
    "ds_read_b128 v[216:219], v254 offset:2048\n\t"
    "v_mfma_f32_16x16x32_bf16 v[240:243], a[236:239], v[80:83], v[240:243]\n\t"
    "v_mfma_f32_16x16x32_bf16 v[240:243], a[240:243], v[104:107], v[240:243]\n\t"
    "v_mfma_f32_16x16x32_bf16 v[240:243], a[244:247], v[128:131], v[240:243]\n\t"
    "buffer_store_dwordx4 v[232:235], v255, %4, s48 offen offset:384 nt sc1\n\t"
    "v_mfma_f32_16x16x32_bf16 v[240:243], a[248:251], v[152:155], v[240:243]\n\t"
    "v_mul_f32_e32 v220, v220, v246\n\t"
    "v_mul_f32_e32 v221, v221, v246\n\t"
    "v_mul_f32_e32 v222, v222, v246\n\t"
    "v_mfma_f32_16x16x32_bf16 v[240:243], a[252:255], v[176:179], v[240:243]\n\t"
    "s_barrier\n\t"
    "v_mul_f32_e32 v223, v223, v246\n\t"
    "v_cvt_pk_bf16_f32 v220, v220, v221\n\t"
    "v_mfma_f32_16x16x32_bf16 v[200:203], a[192:195], v[12:15], v[200:203]\n\t"
    "v_cvt_pk_bf16_f32 v221, v222, v223\n\t"
    "v_mfma_f32_16x16x32_bf16 v[200:203], a[196:199], v[36:39], v[200:203]\n\t"
    "ds_read_b128 v[224:227], v254 offset:3072\n\t"
    "v_mfma_f32_16x16x32_bf16 v[200:203], a[200:203], v[60:63], v[200:203]\n\t"
    "v_mfma_f32_16x16x32_bf16 v[200:203], a[204:207], v[84:87], v[200:203]\n\t"
    "v_mul_f32_e32 v240, v240, v246\n\t"
    "v_mul_f32_e32 v241, v241, v246\n\t"
    "v_mfma_f32_16x16x32_bf16 v[200:203], a[208:211], v[108:111], v[200:203]\n\t"
    "v_mul_f32_e32 v242, v242, v246\n\t"
    "v_mul_f32_e32 v243, v243, v246\n\t"
    "v_mfma_f32_16x16x32_bf16 v[200:203], a[212:215], v[132:135], v[200:203]\n\t"
    "v_cvt_pk_bf16_f32 v222, v240, v241\n\t"
    "v_cvt_pk_bf16_f32 v223, v242, v243\n\t"
    "v_mfma_f32_16x16x32_bf16 v[200:203], a[216:219], v[156:159], v[200:203]\n\t"
    "v_permlane16_swap_b32_e32 v220, v222\n\t"
    "v_mfma_f32_16x16x32_bf16 v[200:203], a[220:223], v[180:183], v[200:203]\n\t"
    "s_waitcnt lgkmcnt(1)\n\t"
    "v_permlane16_swap_b32_e32 v221, v223\n\t"
    "v_mfma_f32_16x16x32_bf16 v[192:195], a[224:227], v[12:15], v[192:195]\n\t"
    "v_mfma_f32_16x16x32_bf16 v[192:195], a[228:231], v[36:39], v[192:195]\n\t"
    "ds_read_b128 v[228:231], v254 offset:4096\n\t"
    "v_mfma_f32_16x16x32_bf16 v[192:195], a[232:235], v[60:63], v[192:195]\n\t"
    "v_mfma_f32_16x16x32_bf16 v[192:195], a[236:239], v[84:87], v[192:195]\n\t"
    "v_mfma_f32_16x16x32_bf16 v[192:195], a[240:243], v[108:111], v[192:195]\n\t"
    "buffer_store_dwordx4 v[220:223], v255, %4, s49 offen offset:384 nt sc1\n\t"
    "v_mfma_f32_16x16x32_bf16 v[192:195], a[244:247], v[132:135], v[192:195]\n\t"
    "v_mul_f32_e32 v200, v200, v247\n\t"
    "v_mul_f32_e32 v201, v201, v247\n\t"
    "v_mfma_f32_16x16x32_bf16 v[192:195], a[248:251], v[156:159], v[192:195]\n\t"
    "v_mul_f32_e32 v202, v202, v247\n\t"
    "v_mul_f32_e32 v203, v203, v247\n\t"
    "v_mfma_f32_16x16x32_bf16 v[192:195], a[252:255], v[180:183], v[192:195]\n\t"
    "v_cvt_pk_bf16_f32 v200, v200, v201\n\t"
    "v_cvt_pk_bf16_f32 v201, v202, v203\n\t"
    "v_mfma_f32_16x16x32_bf16 v[216:219], a[192:195], v[16:19], v[216:219]\n\t"
    "v_mfma_f32_16x16x32_bf16 v[216:219], a[196:199], v[40:43], v[216:219]\n\t"
    "ds_read_b128 v[212:215], v254 offset:5120\n\t"
    "v_mfma_f32_16x16x32_bf16 v[216:219], a[200:203], v[64:67], v[216:219]\n\t"
    "v_mfma_f32_16x16x32_bf16 v[216:219], a[204:207], v[88:91], v[216:219]\n\t"
    "v_mfma_f32_16x16x32_bf16 v[216:219], a[208:211], v[112:115], v[216:219]\n\t"
    "v_mul_f32_e32 v192, v192, v247\n\t"
    "v_mul_f32_e32 v193, v193, v247\n\t"
    "v_mfma_f32_16x16x32_bf16 v[216:219], a[212:215], v[136:139], v[216:219]\n\t"
    "v_mul_f32_e32 v194, v194, v247\n\t"
    "v_mul_f32_e32 v195, v195, v247\n\t"
    "v_mfma_f32_16x16x32_bf16 v[216:219], a[216:219], v[160:163], v[216:219]\n\t"
    "v_cvt_pk_bf16_f32 v202, v192, v193\n\t"
    "v_cvt_pk_bf16_f32 v203, v194, v195\n\t"
    "v_mfma_f32_16x16x32_bf16 v[216:219], a[220:223], v[184:187], v[216:219]\n\t"
    "s_waitcnt lgkmcnt(1)\n\t"
    "v_permlane16_swap_b32_e32 v200, v202\n\t"
    "v_mfma_f32_16x16x32_bf16 v[224:227], a[224:227], v[16:19], v[224:227]\n\t"
    "v_permlane16_swap_b32_e32 v201, v203\n\t"
    "v_mfma_f32_16x16x32_bf16 v[224:227], a[228:231], v[40:43], v[224:227]\n\t"
    "v_mfma_f32_16x16x32_bf16 v[224:227], a[232:235], v[64:67], v[224:227]\n\t"
    "v_mfma_f32_16x16x32_bf16 v[224:227], a[236:239], v[88:91], v[224:227]\n\t"
    "v_mfma_f32_16x16x32_bf16 v[224:227], a[240:243], v[112:115], v[224:227]\n\t"
    "v_mfma_f32_16x16x32_bf16 v[224:227], a[244:247], v[136:139], v[224:227]\n\t"
    "buffer_store_dwordx4 v[200:203], v255, %4, s50 offen offset:384 nt sc1\n\t"
    "v_mfma_f32_16x16x32_bf16 v[224:227], a[248:251], v[160:163], v[224:227]\n\t"
    "v_mul_f32_e32 v216, v216, v248\n\t"
    "v_mul_f32_e32 v217, v217, v248\n\t"
    "v_mul_f32_e32 v218, v218, v248\n\t"
    "v_mfma_f32_16x16x32_bf16 v[224:227], a[252:255], v[184:187], v[224:227]\n\t"
    "v_mul_f32_e32 v219, v219, v248\n\t"
    "v_cvt_pk_bf16_f32 v216, v216, v217\n\t"
    "v_mfma_f32_16x16x32_bf16 v[228:231], a[192:195], v[20:23], v[228:231]\n\t"
    "v_cvt_pk_bf16_f32 v217, v218, v219\n\t"
    "v_mfma_f32_16x16x32_bf16 v[228:231], a[196:199], v[44:47], v[228:231]\n\t"
    "v_mfma_f32_16x16x32_bf16 v[228:231], a[200:203], v[68:71], v[228:231]\n\t"
    "v_mfma_f32_16x16x32_bf16 v[228:231], a[204:207], v[92:95], v[228:231]\n\t"
    "v_mfma_f32_16x16x32_bf16 v[228:231], a[208:211], v[116:119], v[228:231]\n\t"
    "v_mul_f32_e32 v224, v224, v248\n\t"
    "v_mul_f32_e32 v225, v225, v248\n\t"
    "v_mfma_f32_16x16x32_bf16 v[228:231], a[212:215], v[140:143], v[228:231]\n\t"
    "v_mul_f32_e32 v226, v226, v248\n\t"
    "v_mul_f32_e32 v227, v227, v248\n\t"
    "v_mfma_f32_16x16x32_bf16 v[228:231], a[216:219], v[164:167], v[228:231]\n\t"
    "v_cvt_pk_bf16_f32 v218, v224, v225\n\t"
    "v_cvt_pk_bf16_f32 v219, v226, v227\n\t"
    "v_mfma_f32_16x16x32_bf16 v[228:231], a[220:223], v[188:191], v[228:231]\n\t"
    "s_waitcnt lgkmcnt(0)\n\t"
    "v_permlane16_swap_b32_e32 v216, v218\n\t"
    "v_mfma_f32_16x16x32_bf16 v[212:215], a[224:227], v[20:23], v[212:215]\n\t"
    "v_permlane16_swap_b32_e32 v217, v219\n\t"
    "v_mfma_f32_16x16x32_bf16 v[212:215], a[228:231], v[44:47], v[212:215]\n\t"
    "v_mfma_f32_16x16x32_bf16 v[212:215], a[232:235], v[68:71], v[212:215]\n\t"
    "v_mfma_f32_16x16x32_bf16 v[212:215], a[236:239], v[92:95], v[212:215]\n\t"
    "v_mfma_f32_16x16x32_bf16 v[212:215], a[240:243], v[116:119], v[212:215]\n\t"
    "v_mfma_f32_16x16x32_bf16 v[212:215], a[244:247], v[140:143], v[212:215]\n\t"
    "buffer_store_dwordx4 v[216:219], v255, %4, s51 offen offset:384 nt sc1\n\t"
    "v_mfma_f32_16x16x32_bf16 v[212:215], a[248:251], v[164:167], v[212:215]\n\t"
    "v_mul_f32_e32 v228, v228, v249\n\t"
    "v_mul_f32_e32 v229, v229, v249\n\t"
    "v_mul_f32_e32 v230, v230, v249\n\t"
    "v_mfma_f32_16x16x32_bf16 v[212:215], a[252:255], v[188:191], v[212:215]\n\t"
    "v_mul_f32_e32 v231, v231, v249\n\t"
    "v_cvt_pk_bf16_f32 v228, v228, v229\n\t"
    "v_cvt_pk_bf16_f32 v229, v230, v231\n\t"
    "s_nop 4\n\t"
    "v_mul_f32_e32 v212, v212, v249\n\t"
    "v_mul_f32_e32 v213, v213, v249\n\t"
    "v_mul_f32_e32 v214, v214, v249\n\t"
    "v_mul_f32_e32 v215, v215, v249\n\t"
    "v_cvt_pk_bf16_f32 v230, v212, v213\n\t"
    "v_cvt_pk_bf16_f32 v231, v214, v215\n\t"
    "s_nop 0\n\t"
    "v_permlane16_swap_b32_e32 v228, v230\n\t"
    "v_permlane16_swap_b32_e32 v229, v231\n\t"
    "buffer_store_dwordx4 v[228:231], v255, %4, s52 offen offset:384 nt sc1\n\t"
    : "=&s"(flag)
    : "s"(sq), "s"(sk), "s"(sv), "s"(so), "s"(wid)
      , "s"((bid >> 3) + 64 * wid), "s"(((grp + (grp >> 3)) & 7) * 32768), "s"(0)
    : "v0", "v1", "v2", "v3", "v4", "v5", "v6", "v7", "v8", "v9", "v10", "v11", "v12", "v13", "v14", "v15", "v16", "v17", "v18", "v19", "v20", "v21", "v22", "v23", "v24", "v25", "v26", "v27", "v28", "v29", "v30", "v31", "v32", "v33", "v34", "v35", "v36", "v37", "v38", "v39", "v40", "v41", "v42", "v43", "v44", "v45", "v46", "v47", "v48", "v49", "v50", "v51", "v52", "v53", "v54", "v55", "v56", "v57", "v58", "v59", "v60", "v61", "v62", "v63", "v64", "v65", "v66", "v67", "v68", "v69", "v70", "v71", "v72", "v73", "v74", "v75", "v76", "v77", "v78", "v79", "v80", "v81", "v82", "v83", "v84", "v85", "v86", "v87", "v88", "v89", "v90", "v91", "v92", "v93", "v94", "v95", "v96", "v97", "v98", "v99", "v100", "v101", "v102", "v103", "v104", "v105", "v106", "v107", "v108", "v109", "v110", "v111", "v112", "v113", "v114", "v115", "v116", "v117", "v118", "v119", "v120", "v121", "v122", "v123", "v124", "v125", "v126", "v127", "v128", "v129", "v130", "v131", "v132", "v133", "v134", "v135", "v136", "v137", "v138", "v139", "v140", "v141", "v142", "v143", "v144", "v145", "v146", "v147", "v148", "v149", "v150", "v151", "v152", "v153", "v154", "v155", "v156", "v157", "v158", "v159", "v160", "v161", "v162", "v163", "v164", "v165", "v166", "v167", "v168", "v169", "v170", "v171", "v172", "v173", "v174", "v175", "v176", "v177", "v178", "v179", "v180", "v181", "v182", "v183", "v184", "v185", "v186", "v187", "v188", "v189", "v190", "v191", "v192", "v193", "v194", "v195", "v196", "v197", "v198", "v199", "v200", "v201", "v202", "v203", "v204", "v205", "v206", "v207", "v208", "v209", "v210", "v211", "v212", "v213", "v214", "v215", "v216", "v217", "v218", "v219", "v220", "v221", "v222", "v223", "v224", "v225", "v226", "v227", "v228", "v229", "v230", "v231", "v232", "v233", "v234", "v235", "v236", "v237", "v238", "v239", "v240", "v241", "v242", "v243", "v244", "v245", "v246", "v247", "v248", "v249", "v250", "v251", "v252", "v253", "v254", "v255", "a0", "a1", "a2", "a3", "a4", "a5", "a6", "a7", "a8", "a9", "a10", "a11", "a12", "a13", "a14", "a15", "a16", "a17", "a18", "a19", "a20", "a21", "a22", "a23", "a24", "a25", "a26", "a27", "a28", "a29", "a30", "a31", "a32", "a33", "a34", "a35", "a36", "a37", "a38", "a39", "a40", "a41", "a42", "a43", "a44", "a45", "a46", "a47", "a48", "a49", "a50", "a51", "a52", "a53", "a54", "a55", "a56", "a57", "a58", "a59", "a60", "a61", "a62", "a63", "a64", "a65", "a66", "a67", "a68", "a69", "a70", "a71", "a72", "a73", "a74", "a75", "a76", "a77", "a78", "a79", "a80", "a81", "a82", "a83", "a84", "a85", "a86", "a87", "a88", "a89", "a90", "a91", "a92", "a93", "a94", "a95", "a96", "a97", "a98", "a99", "a100", "a101", "a102", "a103", "a104", "a105", "a106", "a107", "a108", "a109", "a110", "a111", "a112", "a113", "a114", "a115", "a116", "a117", "a118", "a119", "a120", "a121", "a122", "a123", "a124", "a125", "a126", "a127", "a128", "a129", "a130", "a131", "a132", "a133", "a134", "a135", "a136", "a137", "a138", "a139", "a140", "a141", "a142", "a143", "a144", "a145", "a146", "a147", "a148", "a149", "a150", "a151", "a152", "a153", "a154", "a155", "a156", "a157", "a158", "a159", "a160", "a161", "a162", "a163", "a164", "a165", "a166", "a167", "a168", "a169", "a170", "a171", "a172", "a173", "a174", "a175", "a176", "a177", "a178", "a179", "a180", "a181", "a182", "a183", "a184", "a185", "a186", "a187", "a188", "a189", "a190", "a191", "a192", "a193", "a194", "a195", "a196", "a197", "a198", "a199", "a200", "a201", "a202", "a203", "a204", "a205", "a206", "a207", "a208", "a209", "a210", "a211", "a212", "a213", "a214", "a215", "a216", "a217", "a218", "a219", "a220", "a221", "a222", "a223", "a224", "a225", "a226", "a227", "a228", "a229", "a230", "a231", "a232", "a233", "a234", "a235", "a236", "a237", "a238", "a239", "a240", "a241", "a242", "a243", "a244", "a245", "a246", "a247", "a248", "a249", "a250", "a251", "a252", "a253", "a254", "a255", "s36", "s37", "s38", "s39", "s40", "s41", "s42", "s43", "s44", "s45", "s46", "s47", "s48", "s49", "s50", "s51", "s52", "s53", "s54", "s55", "s56", "s57", "s58", "s59", "s60", "s61", "s62", "s63", "s64", "s65", "s66", "s67", "s68", "s69", "s70", "vcc", "scc", "memory");
  if (__builtin_expect(flag != 0, 0)) {
    asm volatile("s_waitcnt vmcnt(0) lgkmcnt(0)" ::: "memory");
    slow_path(Q, K, V, O, lds, grp, sub, wid);
  }
}

extern "C" void kernel_launch(void* const* d_in, const int* in_sizes, int n_in,
                              void* d_out, int out_size, void* d_ws, size_t ws_size,
                              hipStream_t stream) {
  (void)hipFuncSetAttribute((const void*)gqa_attn, hipFuncAttributeMaxDynamicSharedMemorySize, 163840);
  gqa_attn<<<256, 256, 163840, stream>>>((const char*)d_in[0], (const char*)d_in[1], (const char*)d_in[2], (char*)d_out);
}
```
